```python
import torch

def _softmax(x, axis=-1):
    return torch.softmax(x, dim=axis)
def _gelu(x):
    return torch.nn.functional.gelu(x, approximate="tanh")
def _silu(x):
    return torch.nn.functional.silu(x)
def _sigmoid(x):
    return torch.sigmoid(x)
def _layernorm(x, axis=-1, eps=1e-5):
    m = x.mean(dim=axis, keepdim=True)
    v = x.var(dim=axis, keepdim=True, unbiased=False)
    return (x - m) / torch.sqrt(v + eps)
def _scatter_add(tgt, idx, src, axis=0):
    return tgt.clone().index_add_(axis, idx, src)
def _segment_sum(src, seg, n):
    out = torch.zeros((n,) + src.shape[1:], dtype=src.dtype)
    return out.index_add_(0, seg, src)
def _pad(x, pw):
    flat = tuple(v for pair in reversed(pw) for v in pair)
    return torch.nn.functional.pad(x, flat)

kernel_name = 'recurrent_scan__gqa_block_cached__mixer_token_mlp__decode'

def setup_inputs():

    _w = (lambda a: (lambda t: t.to(torch.bfloat16) if t.is_floating_point() else t)(torch.as_tensor(a)))
    def _A_setup():
        B, Sq, D = 1, 1, 512
        Hq, Hkv, Sk, Dh = 32, 8, 65536, 128
        assert Hq % Hkv == 0, (Hq, Hkv)
        return {
            "x": _w((lambda *s: torch.randn(*s, dtype=torch.bfloat16) * 0.4)(B, Sq, D)),
            "k_cache": _w((lambda *s: torch.randn(*s, dtype=torch.bfloat16) * 3.2)(B, Hkv, Sk, Dh)),
            "v_cache": _w((lambda *s: torch.randn(*s, dtype=torch.bfloat16) * 1.6)(B, Hkv, Sk, Dh)),
            "wq": _w((lambda *s: torch.randn(*s, dtype=torch.bfloat16) * 3.2)(D, Hq * Dh) * (D ** -0.5)),
            "wo": _w((lambda *s: torch.randn(*s, dtype=torch.bfloat16) * 1.6)(Hq * Dh, D) * ((Hq * Dh) ** -0.5)),
        }
    _A_in = _A_setup()

    def _B_setup():
        B, S, D, F = 1, 1, 512, 1792
        return {
            "x": _w((lambda *s: torch.randn(*s, dtype=torch.bfloat16) * 0.4)(B, S, D)),
            "w1": _w((lambda *s: torch.randn(*s, dtype=torch.bfloat16) * 1.6)(S, F) * (S ** -0.5)),
            "b1": _w((lambda *s: torch.randn(*s, dtype=torch.bfloat16) * 1.6)(F) * 0.02),
            "w2": _w((lambda *s: torch.randn(*s, dtype=torch.bfloat16) * 1.6)(F, S) * (F ** -0.5)),
            "b2": _w((lambda *s: torch.randn(*s, dtype=torch.bfloat16) * 1.6)(S) * 0.02),
        }
    _B_in = _B_setup()

    return {
        "A_x": _A_in["x"],
        "A_k_cache": _A_in["k_cache"],
        "A_v_cache": _A_in["v_cache"],
        "A_wq": _A_in["wq"],
        "A_wo": _A_in["wo"],
        "B_x": _B_in["x"],
        "B_w1": _B_in["w1"],
        "B_b1": _B_in["b1"],
        "B_w2": _B_in["w2"],
        "B_b2": _B_in["b2"],
    }

def reference(A_x, A_k_cache, A_v_cache, A_wq, A_wo, B_x, B_w1, B_b1, B_w2, B_b2):
    def _A_ref(x, k_cache, v_cache, wq, wo):
        B, Sq, D = x.shape
        Hq, Dh = 32, 128
        Hkv = k_cache.shape[1]
        rep = Hq // Hkv
        q = (lambda x, *d: x.permute(*d))((x @ wq).reshape(B, Sq, Hq, Dh), 0, 2, 1, 3)
        k = (lambda x, n, axis: torch.repeat_interleave(x, n, dim=axis))(k_cache, rep, axis=1)
        v = (lambda x, n, axis: torch.repeat_interleave(x, n, dim=axis))(v_cache, rep, axis=1)
        scores = torch.einsum("bhqd,bhkd->bhqk", q, k) * (Dh ** -0.5)
        p = _softmax(scores, axis=-1)
        o = torch.einsum("bhqk,bhkd->bhqd", p, v)
        return (lambda x, *d: x.permute(*d))(o, 0, 2, 1, 3).reshape(B, Sq, Hq * Dh) @ wo

    def _B_ref(x, w1, b1, w2, b2):
        B, S, D = x.shape
        h = _layernorm(x, axis=-1)
        u = torch.einsum("bsd,sf->bdf", h, w1) + b1
        u = _gelu(u)
        y = torch.einsum("bdf,fs->bds", u, w2) + b2
        return (lambda x, *d: x.permute(*d))(y, 0, 2, 1)

    x = A_x
    xn = _layernorm(x, axis=-1)
    s = _A_ref(xn, A_k_cache, A_v_cache, A_wq, A_wo)
    g = _silu(_B_ref(xn, B_w1, B_b1, B_w2, B_b2))
    return x + s * g
```

```cpp
#include <hip/hip_runtime.h>
#include <hip/hip_bf16.h>
#include <cstdint>

#define MIMIC 1

typedef unsigned short u16;
using bf16x2 = __attribute__((ext_vector_type(2))) __bf16;
using u32x4  = __attribute__((ext_vector_type(4))) unsigned;
using f32x4  = __attribute__((ext_vector_type(4))) float;

constexpr int D = 512, HQ = 32, HKV = 8, SK = 65536, DH = 128, F = 1792;
constexpr int NSPLIT = 64;

__device__ __forceinline__ float bf2f(u16 b) { return __uint_as_float(((unsigned)b) << 16); }
__device__ __forceinline__ float rbf(float f) {
  unsigned u = __float_as_uint(f);
  u += 0x7FFFu + ((u >> 16) & 1u);
  return __uint_as_float(u & 0xFFFF0000u);
}
__device__ __forceinline__ float R(float f) { return MIMIC ? rbf(f) : f; }
__device__ __forceinline__ u16 f2bf(float f) {
  unsigned u = __float_as_uint(f);
  u += 0x7FFFu + ((u >> 16) & 1u);
  return (u16)(u >> 16);
}
__device__ __forceinline__ float dot2(unsigned k, unsigned q, float acc) {
  return __builtin_amdgcn_fdot2_f32_bf16(__builtin_bit_cast(bf16x2, k), __builtin_bit_cast(bf16x2, q), acc, false);
}
template <int CTRL> __device__ __forceinline__ float dpp(float x) {
  return __builtin_bit_cast(float, __builtin_amdgcn_mov_dpp(__builtin_bit_cast(int, x), CTRL, 0xf, 0xf, true));
}
constexpr int XOR1 = 0xB1, XOR2 = 0x4E, XOR7 = 0x141, XOR8 = 0x128, ROR4 = 0x124;
__device__ __forceinline__ float xrow16_max(float x) {
  auto s = __builtin_amdgcn_permlane16_swap(__float_as_uint(x), __float_as_uint(x), false, false);
  x = fmaxf(__uint_as_float(s[0]), __uint_as_float(s[1]));
  auto t = __builtin_amdgcn_permlane32_swap(__float_as_uint(x), __float_as_uint(x), false, false);
  return fmaxf(__uint_as_float(t[0]), __uint_as_float(t[1]));
}
__device__ __forceinline__ float xrow16_sum(float x) {
  auto s = __builtin_amdgcn_permlane16_swap(__float_as_uint(x), __float_as_uint(x), false, false);
  x = __uint_as_float(s[0]) + __uint_as_float(s[1]);
  auto t = __builtin_amdgcn_permlane32_swap(__float_as_uint(x), __float_as_uint(x), false, false);
  return __uint_as_float(t[0]) + __uint_as_float(t[1]);
}
__device__ __forceinline__ float wave_sum(float v) {
  for (int m = 32; m >= 1; m >>= 1) v += __shfl_xor(v, m);
  return v;
}

__device__ void layernorm512(float* xs  , int tid) {
  const int lane = tid & 63;
  float s = 0.f;
  for (int i = lane; i < D; i += 64) s += xs[i];
  s = wave_sum(s);
  const float mean = s * (1.f / D);
  float v = 0.f;
  for (int i = lane; i < D; i += 64) { float d = xs[i] - mean; v += d * d; }
  v = wave_sum(v) * (1.f / D);
  const float m = R(mean), vv = R(v);
  const float den = R(sqrtf(R(vv + 1e-5f)));
  __syncthreads();
  for (int i = tid; i < D; i += blockDim.x) xs[i] = R(R(xs[i] - m) / den);
  __syncthreads();
}

constexpr size_t WS_Q = 0, WS_G = 16384, WS_PML = 32768, WS_PO = WS_PML + HQ * NSPLIT * 2 * 4;

__global__ void __launch_bounds__(256) k_pre(const u16* x, const u16* wq, const u16* w1, const u16* b1, const u16* w2, const u16* b2, unsigned char* ws) {
  __shared__ float xs[D];
  __shared__ float hs[D];
  __shared__ float red[256 * 8];
  const int tid = threadIdx.x, b = blockIdx.x;
  for (int i = tid; i < D; i += 256) xs[i] = bf2f(x[i]);
  __syncthreads();
  layernorm512(xs, tid);
  {
    const int half = tid & 1, rg = tid >> 1;
    float acc[8] = {0};
    for (int i = 0; i < 4; ++i) {
      const int r = rg + 128 * i;
      u32x4 w = *reinterpret_cast<const u32x4*>(wq + (size_t)r * 4096 + 16 * b + 8 * half);
      const float xv = xs[r];
      for (int k = 0; k < 4; ++k) {
        acc[2 * k] = fmaf(xv, __uint_as_float(w[k] << 16), acc[2 * k]);
        acc[2 * k + 1] = fmaf(xv, __uint_as_float(w[k] & 0xffff0000u), acc[2 * k + 1]);
      }
    }
    for (int k = 0; k < 8; ++k) red[tid * 8 + k] = acc[k];
    __syncthreads();
    if (tid < 16) {
      const int half2 = tid >> 3, k = tid & 7;
      float s = 0.f;
      for (int g = 0; g < 128; ++g) s += red[(g * 2 + half2) * 8 + k];
      reinterpret_cast<float*>(ws + WS_Q)[16 * b + tid] = R(s);
    }
  }
  for (int i = tid; i < D; i += 256) hs[i] = xs[i];
  __syncthreads();
  layernorm512(hs, tid);
  {
    const int dd = tid >> 7, t = tid & 127;
    const float h = hs[2 * b + dd];
    float acc = 0.f;
    for (int f = t; f < F; f += 128) {
      float u = R(R(h * bf2f(w1[f])) + bf2f(b1[f]));
      float inner = 0.7978845608028654f * (u + 0.044715f * u * u * u);
      float ge = R(0.5f * u * (1.f + tanhf(inner)));
      acc = fmaf(ge, bf2f(w2[f]), acc);
    }
    acc = wave_sum(acc);
    __syncthreads();
    if ((tid & 63) == 0) red[tid >> 6] = acc;
    __syncthreads();
    if (tid < 2) {
      float y = R(R(red[2 * tid] + red[2 * tid + 1]) + bf2f(b2[0]));
      float g = R(y / (1.f + __expf(-y)));
      reinterpret_cast<float*>(ws + WS_G)[2 * b + tid] = g;
    }
  }
}

__global__ void __launch_bounds__(256) k_attn(const u16* kc, const u16* vc, unsigned char* ws) {
  constexpr int PPS = SK / 16 / NSPLIT, PPW = PPS / 4;
  __shared__ __attribute__((aligned(16))) float lds[4 * 64 + 4 * 4 * (4 + DH)];
  const int s = blockIdx.x, kvh = blockIdx.y;
  const int tid = threadIdx.x, lane = tid & 63, wave = __builtin_amdgcn_readfirstlane(tid >> 6), r = lane >> 4, c = lane & 15;
  constexpr float SCALE = 0.08838834764831845f;
  constexpr float LOG2E = 1.4426950408889634f;
  const float* qf = reinterpret_cast<const float*>(ws + WS_Q);
  u32x4 qpk[4], qlo[4];
  for (int g = 0; g < 4; ++g) {
    const float* qp = qf + (kvh * 4 + g) * DH + 8 * c;
    for (int k = 0; k < 4; ++k) {
      float a = qp[2 * k], bb = qp[2 * k + 1];
      u16 ah = f2bf(a), bh = f2bf(bb);
      qpk[g][k] = (unsigned)ah | ((unsigned)bh << 16);
      u16 al = f2bf(a - bf2f(ah)), bl = f2bf(bb - bf2f(bh));
      qlo[g][k] = (unsigned)al | ((unsigned)bl << 16);
    }
  }
  const size_t base = ((size_t)kvh * SK + (size_t)s * PPS * 16) * DH + (size_t)r * DH + 8 * c;
  u32x4 Kn[4], Vn[4];
#define ISSUE(i_) do { const size_t o_ = base + (size_t)(wave + 4 * (i_)) * 16 * DH; \
    _Pragma("unroll") for (int t_ = 0; t_ < 4; ++t_) { Kn[t_] = __builtin_nontemporal_load(reinterpret_cast<const u32x4*>(kc + o_ + t_ * 4 * DH)); Vn[t_] = __builtin_nontemporal_load(reinterpret_cast<const u32x4*>(vc + o_ + t_ * 4 * DH)); } } while (0)
  float m = -INFINITY, l = 0.f, acc[32];
  for (int k = 0; k < 32; ++k) acc[k] = 0.f;
  ISSUE(0);
  for (int i = 0; i < PPW; ++i) {
    u32x4 Kc[4], Vc[4];
#pragma unroll
    for (int t = 0; t < 4; ++t) { Kc[t] = Kn[t]; Vc[t] = Vn[t]; }
    if (i + 1 < PPW) ISSUE(i + 1);
    float x[16];
#pragma unroll
    for (int t = 0; t < 4; ++t)
#pragma unroll
      for (int g = 0; g < 4; ++g) { float d = 0.f;
#pragma unroll
        for (int k = 0; k < 4; ++k) d = dot2(Kc[t][k], qpk[g][k], d);
        if (!MIMIC) {
#pragma unroll
          for (int k = 0; k < 4; ++k) d = dot2(Kc[t][k], qlo[g][k], d);
        }
        x[t * 4 + g] = d; }
#define TR_STEP(HALF, CTRL, BIT) _Pragma("unroll") for (int h = 0; h < (HALF); ++h) { \
      const float lo_ = x[h], hi_ = x[h + (HALF)]; const float t1_ = lo_ + dpp<CTRL>(lo_), t2_ = hi_ + dpp<CTRL>(hi_); \
      x[h] = (c & (BIT)) ? t2_ : t1_; }
    TR_STEP(8, XOR8, 8) TR_STEP(4, XOR7, 4) TR_STEP(2, XOR2, 2) TR_STEP(1, XOR1, 1)
#undef TR_STEP
    const float sc = R(R(x[0]) * SCALE) * LOG2E;
    float pm = sc; pm = fmaxf(pm, dpp<ROR4>(pm)); pm = fmaxf(pm, dpp<XOR8>(pm)); pm = xrow16_max(pm);
    const float mn = fmaxf(m, pm);
    const float alpha = __builtin_amdgcn_exp2f(m - mn), p = __builtin_amdgcn_exp2f(sc - mn);
    l = fmaf(l, alpha, p); m = mn;
#pragma unroll
    for (int q4 = 0; q4 < 4; ++q4) {
      const float al = q4 == 0 ? dpp<0x00>(alpha) : q4 == 1 ? dpp<0x55>(alpha) : q4 == 2 ? dpp<0xAA>(alpha) : dpp<0xFF>(alpha);
#pragma unroll
      for (int k = 0; k < 8; ++k) acc[q4 * 8 + k] *= al; }
    float* prow = lds + (wave * 4 + r) * 16;
    prow[c] = p;
    asm volatile("s_waitcnt lgkmcnt(0)" ::: "memory");
    f32x4 pr[4];
#pragma unroll
    for (int u = 0; u < 4; ++u) pr[u] = *reinterpret_cast<const f32x4*>(prow + 4 * u);
#pragma unroll
    for (int t = 0; t < 4; ++t) {
      float vf[8];
#pragma unroll
      for (int k = 0; k < 4; ++k) { vf[2 * k] = __uint_as_float(Vc[t][k] << 16); vf[2 * k + 1] = __uint_as_float(Vc[t][k] & 0xffff0000u); }
#pragma unroll
      for (int g = 0; g < 4; ++g) { const float pg_ = pr[t][g];
#pragma unroll
        for (int k = 0; k < 8; ++k) acc[g * 8 + k] = fmaf(pg_, vf[k], acc[g * 8 + k]); } }
  }
#undef ISSUE
#pragma unroll
  for (int k = 0; k < 32; ++k) acc[k] = xrow16_sum(acc[k]);
  { float t = l; t += dpp<ROR4>(t); t += dpp<XOR8>(t); l = xrow16_sum(t); }
  float* mrg = lds + 256;
  constexpr int REC = 4 + DH;
  if (r == 0) {
#pragma unroll
    for (int g = 0; g < 4; ++g) {
      float* rec = mrg + (wave * 4 + g) * REC;
#pragma unroll
      for (int k = 0; k < 8; k += 4) *reinterpret_cast<f32x4*>(rec + 4 + 8 * c + k) = f32x4{acc[g * 8 + k], acc[g * 8 + k + 1], acc[g * 8 + k + 2], acc[g * 8 + k + 3]};
      if (c == g) { rec[0] = m; rec[1] = l; } } }
  __syncthreads();
  float* pml = reinterpret_cast<float*>(ws + WS_PML);
  float* po = reinterpret_cast<float*>(ws + WS_PO);
  {
    const int g = wave, d0 = 2 * lane;
    const int row = kvh * 4 + g;
    float M = -INFINITY;
    for (int w = 0; w < 4; ++w) M = fmaxf(M, mrg[(w * 4 + g) * REC]);
    float L = 0.f, o0 = 0.f, o1 = 0.f;
    for (int w = 0; w < 4; ++w) { const float* rec = mrg + (w * 4 + g) * REC; const float lw = rec[1];
      const float wt = __builtin_amdgcn_exp2f(rec[0] - M);
      L = fmaf(wt, lw, L); o0 = fmaf(wt, rec[4 + d0], o0); o1 = fmaf(wt, rec[4 + d0 + 1], o1); }
    const size_t slot = (size_t)row * NSPLIT + s;
    *reinterpret_cast<float2*>(po + slot * DH + d0) = make_float2(o0, o1);
    if (lane == 0) *reinterpret_cast<float2*>(pml + 2 * slot) = make_float2(M, L);
  }
}

constexpr size_t WS_O = WS_PO + (size_t)HQ * NSPLIT * DH * 4;
__global__ void __launch_bounds__(64) k_comb(unsigned char* ws) {
  const int row = blockIdx.x, d0 = 2 * threadIdx.x;
  const float* pml = reinterpret_cast<const float*>(ws + WS_PML) + (size_t)row * NSPLIT * 2;
  const float* po = reinterpret_cast<const float*>(ws + WS_PO) + (size_t)row * NSPLIT * DH;
  float M = -INFINITY;
  for (int s = 0; s < NSPLIT; ++s) M = fmaxf(M, pml[2 * s]);
  float L = 0.f, o0 = 0.f, o1 = 0.f;
  for (int s = 0; s < NSPLIT; ++s) {
    const float w = __builtin_amdgcn_exp2f(pml[2 * s] - M);
    const float2 a = *reinterpret_cast<const float2*>(po + (size_t)s * DH + d0);
    L = fmaf(w, pml[2 * s + 1], L); o0 = fmaf(w, a.x, o0); o1 = fmaf(w, a.y, o1);
  }
  float* o = reinterpret_cast<float*>(ws + WS_O) + row * DH;
  o[d0] = R(o0 / L); o[d0 + 1] = R(o1 / L);
}

__global__ void __launch_bounds__(256) k_out(const u16* x, const u16* wo, const unsigned char* ws, u16* out) {
  __shared__ float red[4 * 8];
  const int tid = threadIdx.x, b = blockIdx.x;
  const float* o = reinterpret_cast<const float*>(ws + WS_O);
  float acc[8] = {0};
  for (int i = 0; i < 16; ++i) {
    const int r = tid + 256 * i;
    u32x4 w = *reinterpret_cast<const u32x4*>(wo + (size_t)r * D + 8 * b);
    const float ov = o[r];
    for (int k = 0; k < 4; ++k) {
      acc[2 * k] = fmaf(ov, __uint_as_float(w[k] << 16), acc[2 * k]);
      acc[2 * k + 1] = fmaf(ov, __uint_as_float(w[k] & 0xffff0000u), acc[2 * k + 1]);
    }
  }
  for (int k = 0; k < 8; ++k) acc[k] = wave_sum(acc[k]);
  if ((tid & 63) == 0) for (int k = 0; k < 8; ++k) red[(tid >> 6) * 8 + k] = acc[k];
  __syncthreads();
  if (tid < 8) {
    float s = R(red[tid] + red[8 + tid] + red[16 + tid] + red[24 + tid]);
    const int n = 8 * b + tid;
    const float g = reinterpret_cast<const float*>(ws + WS_G)[n];
    out[n] = f2bf(bf2f(x[n]) + R(s * g));
  }
}

extern "C" void kernel_launch(void* const* d_in, const int* in_sizes, int n_in,
                              void* d_out, int out_size, void* d_ws, size_t ws_size,
                              hipStream_t stream) {
  const u16* x = (const u16*)d_in[0];
  const u16* kc = (const u16*)d_in[1];
  const u16* vc = (const u16*)d_in[2];
  const u16* wq = (const u16*)d_in[3];
  const u16* wo = (const u16*)d_in[4];
  const u16* w1 = (const u16*)d_in[6];
  const u16* b1 = (const u16*)d_in[7];
  const u16* w2 = (const u16*)d_in[8];
  const u16* b2 = (const u16*)d_in[9];
  unsigned char* ws = (unsigned char*)d_ws;
  k_pre<<<256, 256, 0, stream>>>(x, wq, w1, b1, w2, b2, ws);
  k_attn<<<dim3(NSPLIT, HKV), 256, 0, stream>>>(kc, vc, ws);
  k_comb<<<HQ, 64, 0, stream>>>(ws);
  k_out<<<64, 256, 0, stream>>>(x, wo, ws, (u16*)d_out);
}
```

```cpp
#include <hip/hip_runtime.h>
#include <cstdint>

#define GAS __attribute__((address_space(1)))
#define LAS __attribute__((address_space(3)))
typedef unsigned short u16;
typedef unsigned v4u __attribute__((ext_vector_type(4)));
typedef unsigned v2u __attribute__((ext_vector_type(2)));
typedef float f32x4 __attribute__((ext_vector_type(4)));
typedef float f32x2 __attribute__((ext_vector_type(2)));
typedef short s16x4 __attribute__((ext_vector_type(4)));
typedef __bf16 bf16x8 __attribute__((ext_vector_type(8)));
typedef __bf16 bf16x2 __attribute__((ext_vector_type(2)));

namespace {
constexpr int D = 512, HQ = 32, HKV = 8, SK = 65536, DH = 128, FF = 1792;
constexpr int NWG = 256, NC = 4, NWAVE = NC + 2, NTHR = NWAVE * 64;
constexpr int TK = 32;
constexpr int SLOT = 2 * TK * 256;
constexpr int NSLOT = 9;
constexpr int TPW = SK / 32 / TK;
constexpr int L_RING = 0;
constexpr int L_CTRL = NSLOT * SLOT;
constexpr int C_FULL = 0, C_TOTAL = 1, C_QRDY = 2, C_QCNT = 3, C_MCNT = 4, C_YCNT = 5, C_ABORT = 6, C_FREE = 16;
constexpr int L_Q = L_CTRL + 256;
constexpr int L_QP = L_Q + 1024;
constexpr int L_ML = L_QP + 256;
constexpr int L_Y = L_ML + 128;
constexpr int L_O = L_Y + 1024;
constexpr int LDS_BYTES = L_O + 8192;
static_assert(LDS_BYTES <= 160 * 1024, "LDS");
constexpr unsigned W_EPOCH = 0;
constexpr unsigned W_QG = 4096;
constexpr unsigned W_LSE = W_QG + 8 * 256 * 8;
constexpr unsigned W_P2 = W_LSE + 32 * 32 * 8;
constexpr unsigned W_P1 = W_P2 + 256 * 64 * 8;
constexpr unsigned W_END = W_P1 + 32 * 4 * 32 * 32 * 8;
constexpr unsigned W_STAMP = W_END;
constexpr int AUX_SC1 = 16, AUX_NT = 2;
constexpr unsigned SPIN_CAP = 1u << 22;

__device__ __forceinline__ float bf_lo(unsigned u) { return __uint_as_float(u << 16); }
__device__ __forceinline__ float bf_hi(unsigned u) { return __uint_as_float(u & 0xffff0000u); }
__device__ __forceinline__ unsigned pk_bf16(float lo, float hi) {
  bf16x2 r = {(__bf16)lo, (__bf16)hi};
  return __builtin_bit_cast(unsigned, r);
}
__device__ __forceinline__ float rbf(float f) { return bf_lo(pk_bf16(f, f)); }
template <int CTRL> __device__ __forceinline__ float dpp(float x) {
  return __builtin_bit_cast(float, __builtin_amdgcn_mov_dpp(__builtin_bit_cast(int, x), CTRL, 0xf, 0xf, true));
}
constexpr int XOR1 = 0xB1, XOR2 = 0x4E, XOR7 = 0x141, XOR8 = 0x128;
__device__ __forceinline__ float x16_sum(float x) { auto s = __builtin_amdgcn_permlane16_swap(__float_as_uint(x), __float_as_uint(x), false, false); return __uint_as_float(s[0]) + __uint_as_float(s[1]); }
__device__ __forceinline__ float x32_sum(float x) { auto s = __builtin_amdgcn_permlane32_swap(__float_as_uint(x), __float_as_uint(x), false, false); return __uint_as_float(s[0]) + __uint_as_float(s[1]); }
__device__ __forceinline__ float x16_max(float x) { auto s = __builtin_amdgcn_permlane16_swap(__float_as_uint(x), __float_as_uint(x), false, false); return fmaxf(__uint_as_float(s[0]), __uint_as_float(s[1])); }
__device__ __forceinline__ float x32_max(float x) { auto s = __builtin_amdgcn_permlane32_swap(__float_as_uint(x), __float_as_uint(x), false, false); return fmaxf(__uint_as_float(s[0]), __uint_as_float(s[1])); }
__device__ __forceinline__ float wave_sum(float v) {
  v += dpp<XOR1>(v); v += dpp<XOR2>(v); v += dpp<XOR7>(v); v += dpp<XOR8>(v);
  return x32_sum(x16_sum(v));
}
__device__ __forceinline__ float dot2(unsigned a, unsigned b, float acc) {
  return __builtin_amdgcn_fdot2_f32_bf16(__builtin_bit_cast(bf16x2, a), __builtin_bit_cast(bf16x2, b), acc, false);
}
__device__ __forceinline__ f32x4 mfma16(v4u a, v4u b, f32x4 c) {
  return __builtin_amdgcn_mfma_f32_16x16x32_bf16(__builtin_bit_cast(bf16x8, a), __builtin_bit_cast(bf16x8, b), c, 0, 0, 0);
}
#define GLDS(src, dst, off) __builtin_amdgcn_global_load_lds((const GAS unsigned*)(src), (LAS unsigned*)(dst), 16, (off), AUX_NT)
#define WAIT_VM(n) asm volatile("s_waitcnt vmcnt(" #n ")" ::: "memory")
#define WAIT_LGKM0() asm volatile("s_waitcnt lgkmcnt(0)" ::: "memory")
__device__ __forceinline__ int rfl(int v) { return __builtin_amdgcn_readfirstlane(v); }

__device__ __forceinline__ void ln_stats(const float (&v)[8], float& m, float& den) {
  float s = 0.f;
#pragma unroll
  for (int k = 0; k < 8; ++k) s += v[k];
  const float mean = wave_sum(s) * (1.f / D);
  float q = 0.f;
#pragma unroll
  for (int k = 0; k < 8; ++k) { const float d = v[k] - mean; q = fmaf(d, d, q); }
  const float var = wave_sum(q) * (1.f / D);
  m = rbf(mean);
  den = rbf(sqrtf(rbf(rbf(var) + 1e-5f)));
}
__device__ __forceinline__ float ln_apply(float x, float m, float den) { return rbf(rbf(x - m) / den); }

#define SPIN(cond, sleepn, okvar) do { unsigned sp_ = 0; okvar = true; _Pragma("nounroll") for (;;) { asm volatile("" ::: "memory"); if (cond) break; \
    if (sleepn) __builtin_amdgcn_s_sleep(sleepn); \
    if ((++sp_ & 63u) == 0u && (sp_ > SPIN_CAP || rfl(C[C_ABORT]) != 0)) { okvar = false; break; } } asm volatile("" ::: "memory"); } while (0)

#define STAMP(i) do {} while (0)

__global__ void __launch_bounds__(NTHR, 2) fused_decode(const u16* __restrict__ x, const u16* __restrict__ kc, const u16* __restrict__ vc,
                                                         const u16* __restrict__ wq, const u16* __restrict__ wo, const u16* __restrict__ w1,
                                                         const u16* __restrict__ b1, const u16* __restrict__ w2, const u16* __restrict__ b2,
                                                         u16* __restrict__ out, unsigned char* __restrict__ ws) {
  extern __shared__ __attribute__((aligned(16))) unsigned char smem[];
  LAS unsigned char* L = (LAS unsigned char*)smem;
  volatile LAS int* C = (volatile LAS int*)(L + L_CTRL);
  const int tid = threadIdx.x, lane = tid & 63, wave = rfl(tid >> 6), b = blockIdx.x;
  const int G = b >> 5, gi = b & 31;
  const __amdgpu_buffer_rsrc_t rs = __builtin_amdgcn_make_buffer_rsrc((void*)ws, 0, (int)W_END, 0x00020000);

  if (__builtin_expect(wave == 0, 1)) {
    STAMP(0);
    const GAS unsigned char* kb = (const GAS unsigned char*)kc + ((size_t)G * SK + (size_t)gi * (TPW * TK)) * 256;
    const GAS unsigned char* vb = (const GAS unsigned char*)vc + ((size_t)G * SK + (size_t)gi * (TPW * TK)) * 256;
    const int r4 = lane >> 4, c = lane & 15;
    unsigned ko[4], vo[2];
#pragma unroll
    for (int j = 0; j < 4; ++j) ko[j] = r4 * 256 + ((c ^ (4 * j + r4)) * 16);
#pragma unroll
    for (int j = 0; j < 2; ++j) vo[j] = r4 * 256 + ((c ^ ((4 * j + r4) << 1)) * 16);
    auto issue = [&](int t, int s) {
      const GAS unsigned char* ks = kb + (size_t)t * (TK * 256);
      const GAS unsigned char* vs = vb + (size_t)t * (TK * 256);
      LAS unsigned char* dst = L + s * SLOT;
      GLDS(ks + ko[0], dst, 0); GLDS(ks + ko[1], dst, 1024); GLDS(ks + ko[2], dst, 2048); GLDS(ks + ko[3], dst, 3072);
      GLDS(ks + 4096 + ko[0], dst + 4096, 0); GLDS(ks + 4096 + ko[1], dst + 4096, 1024); GLDS(ks + 4096 + ko[2], dst + 4096, 2048); GLDS(ks + 4096 + ko[3], dst + 4096, 3072);
      GLDS(vs + vo[0], dst + 8192, 0); GLDS(vs + vo[1], dst + 8192, 1024); GLDS(vs + vo[0], dst + 8192, 2048); GLDS(vs + vo[1], dst + 8192, 3072);
      GLDS(vs + 4096 + vo[0], dst + 12288, 0); GLDS(vs + 4096 + vo[1], dst + 12288, 1024); GLDS(vs + 4096 + vo[0], dst + 12288, 2048); GLDS(vs + 4096 + vo[1], dst + 12288, 3072);
    };
    issue(0, 0);
    issue(1, 1);
    issue(2, 2);
    __builtin_amdgcn_s_barrier();
    WAIT_VM(32); C[C_FULL] = 1;
    int pub = 1, s = 3;
    bool dead = false;
    for (int t = 3; t < TPW; ++t) {
      if (t >= NSLOT) {
        const int need = t - NSLOT + 1;
        if (rfl(C[C_FREE + s]) < need) {
          if (pub < t - 1) { WAIT_VM(16); pub = t - 1; C[C_FULL] = pub; }
          WAIT_VM(0); pub = t; C[C_FULL] = pub;
          bool ok; SPIN(rfl(C[C_FREE + s]) >= need, 1, ok);
          if (!ok) { dead = true; break; }
        }
        asm volatile("" ::: "memory");
      }
      issue(t, s);
      if (pub < t - 1) { WAIT_VM(32); pub = t - 1; C[C_FULL] = pub; }
      s = (s + 1 == NSLOT) ? 0 : s + 1;
    }
    if (dead) { WAIT_VM(0); C[C_ABORT] = 1; return; }
    WAIT_VM(16); C[C_FULL] = TPW - 1;
    WAIT_VM(0); C[C_FULL] = TPW;
    STAMP(1);
    return;
  }

  if (wave <= NC) {
    const int cw = wave - 1;
    const int jrow = (lane & 3) | ((lane >> 3) << 2), half = (lane >> 2) & 1;
    const int col0 = G * 512 + ((gi & 7) * 4 + (gi >> 3)) * 16;
    v4u wv[4];
#pragma unroll
    for (int i = 0; i < 4; ++i) wv[i] = __builtin_nontemporal_load((const GAS v4u*)(wq + (size_t)(16 * jrow + 4 * cw + i) * 4096 + col0 + 8 * half));
    const v2u xq = *(const GAS v2u*)(x + 16 * jrow + 4 * cw);
    const v4u xr = *(const GAS v4u*)(x + 8 * lane);
    const unsigned tag = __hip_atomic_load((GAS unsigned*)(ws + W_EPOCH), __ATOMIC_RELAXED, __HIP_MEMORY_SCOPE_AGENT) + 1u;
    if (tid < 64 + 64) { C[lane] = (lane == C_TOTAL) ? TPW : 0; }
    WAIT_LGKM0();
    __builtin_amdgcn_sched_barrier(0);
    __builtin_amdgcn_s_barrier();
    __builtin_amdgcn_sched_barrier(0);
    {
      float xv[8];
#pragma unroll
      for (int k = 0; k < 4; ++k) { xv[2 * k] = bf_lo(xr[k]); xv[2 * k + 1] = bf_hi(xr[k]); }
      float m, den;
      ln_stats(xv, m, den);
      float xn[4] = {ln_apply(bf_lo(xq[0]), m, den), ln_apply(bf_hi(xq[0]), m, den), ln_apply(bf_lo(xq[1]), m, den), ln_apply(bf_hi(xq[1]), m, den)};
      float a[8];
#pragma unroll
      for (int k = 0; k < 8; ++k) a[k] = 0.f;
#pragma unroll
      for (int i = 0; i < 4; ++i)
#pragma unroll
        for (int k = 0; k < 4; ++k) { a[2 * k] = fmaf(xn[i], bf_lo(wv[i][k]), a[2 * k]); a[2 * k + 1] = fmaf(xn[i], bf_hi(wv[i][k]), a[2 * k + 1]); }
      const bool b0 = lane & 1, b1_ = lane & 2, b3 = lane & 8;
#pragma unroll
      for (int k = 0; k < 4; ++k) { const float keep = b0 ? a[k + 4] : a[k], send = b0 ? a[k] : a[k + 4]; a[k] = keep + dpp<XOR1>(send); }
#pragma unroll
      for (int k = 0; k < 2; ++k) { const float keep = b1_ ? a[k + 2] : a[k], send = b1_ ? a[k] : a[k + 2]; a[k] = keep + dpp<XOR2>(send); }
      { const float keep = b3 ? a[1] : a[0], send = b3 ? a[0] : a[1]; a[0] = keep + dpp<XOR8>(send); }
      const float qs = x32_sum(x16_sum(a[0]));
      const int qc = half * 8 + (lane & 1) * 4 + ((lane >> 1) & 1) * 2 + ((lane >> 3) & 1);
      LAS float* QP = (LAS float*)(L + L_QP);
      if (lane < 16) QP[cw * 16 + qc] = qs;
      WAIT_LGKM0();
      int old = 0;
      if (lane == 0) old = __hip_atomic_fetch_add((LAS int*)&C[C_QCNT], 1, __ATOMIC_RELAXED, __HIP_MEMORY_SCOPE_WORKGROUP);
      if (rfl(old) == NC - 1) {
        asm volatile("" ::: "memory");
        if (lane < 8) {
          const f32x2 p0 = *(LAS f32x2*)(QP + 2 * lane), p1 = *(LAS f32x2*)(QP + 16 + 2 * lane), p2 = *(LAS f32x2*)(QP + 32 + 2 * lane), p3 = *(LAS f32x2*)(QP + 48 + 2 * lane);
          const unsigned pq = pk_bf16((p0[0] + p1[0]) + (p2[0] + p3[0]), (p0[1] + p1[1]) + (p2[1] + p3[1]));
          __hip_atomic_store((GAS unsigned long long*)(ws + W_QG) + (col0 >> 1) + lane, ((unsigned long long)tag << 32) | pq, __ATOMIC_RELAXED, __HIP_MEMORY_SCOPE_AGENT);
        }
        STAMP(2);
      }
    }
    const int hp = b >> 3, jc = b & 7, r3 = lane >> 3, p = lane & 7;
    v4u wov[4];
#pragma unroll
    for (int k = 0; k < 4; ++k) wov[k] = __builtin_nontemporal_load((const GAS v4u*)(wo + (size_t)(hp * 128 + 32 * cw + 8 * k + r3) * 512 + 64 * jc + 8 * p));

    const int g4 = lane >> 4, r16 = lane & 15;
    unsigned kaddr[4];
#pragma unroll
    for (int s = 0; s < 4; ++s) kaddr[s] = r16 * 256 + (((4 * s + g4) ^ r16) * 16);
    const int vrow = 4 * g4 + ((lane >> 2) & 3);
    const unsigned vaddr0 = 8192 + vrow * 256 + (vrow & 7) * 32 + (lane & 3) * 8;
    v4u qf[4];
    f32x4 O[8];
#pragma unroll
    for (int d = 0; d < 8; ++d) O[d] = f32x4{0.f, 0.f, 0.f, 0.f};
    float mrun = -INFINITY, lsum = 0.f;
    constexpr float SCALE = 0.08838834764831845f, LOG2E = 1.4426950408889634f;
    bool dead = false;
    int slot = cw;
    for (int t = cw; t < TPW; t += NC) {
      bool ok; SPIN(rfl(C[C_FULL]) > t, 0, ok);
      if (!ok) { dead = true; break; }
      const unsigned sb = slot * SLOT;
      v4u kf[2][4];
      v4u vf[8];
#pragma unroll
      for (int s = 0; s < 4; ++s) {
        kf[0][s] = *(const LAS v4u*)(L + sb + kaddr[s]);
        kf[1][s] = *(const LAS v4u*)(L + sb + kaddr[s] + 4096);
      }
#pragma unroll
      for (int d = 0; d < 8; ++d) {
        const unsigned a = (sb + vaddr0) ^ (d * 32);
        const s16x4 lo = __builtin_amdgcn_ds_read_tr16_b64_v4i16((LAS s16x4*)(L + a));
        const s16x4 hi = __builtin_amdgcn_ds_read_tr16_b64_v4i16((LAS s16x4*)(L + a + 4096));
        const v2u l2 = __builtin_bit_cast(v2u, lo), h2 = __builtin_bit_cast(v2u, hi);
        vf[d] = v4u{l2[0], l2[1], h2[0], h2[1]};
      }
      WAIT_LGKM0();
      C[C_FREE + slot] = t + 1;
      asm volatile("" : "+v"(kf[0][0]), "+v"(kf[0][1]), "+v"(kf[0][2]), "+v"(kf[0][3]), "+v"(kf[1][0]), "+v"(kf[1][1]), "+v"(kf[1][2]), "+v"(kf[1][3]) :: "memory");
      asm volatile("" : "+v"(vf[0]), "+v"(vf[1]), "+v"(vf[2]), "+v"(vf[3]), "+v"(vf[4]), "+v"(vf[5]), "+v"(vf[6]), "+v"(vf[7]) :: "memory");
      slot += NC; if (slot >= NSLOT) slot -= NSLOT;
      if (t == cw) {
        SPIN(rfl(C[C_QRDY]) != 0, 1, ok);
        if (!ok) { dead = true; break; }
#pragma unroll
        for (int s = 0; s < 4; ++s) qf[s] = *(const LAS v4u*)(L + L_Q + (lane & 3) * 256 + s * 64 + g4 * 16);
      }
      f32x4 s0 = {0.f, 0.f, 0.f, 0.f}, s1 = {0.f, 0.f, 0.f, 0.f};
#pragma unroll
      for (int s = 0; s < 4; ++s) { s0 = mfma16(kf[0][s], qf[s], s0); s1 = mfma16(kf[1][s], qf[s], s1); }
      float sc[8];
#pragma unroll
      for (int k = 0; k < 4; k += 2) {
        const unsigned u0 = pk_bf16(s0[k], s0[k + 1]), u1 = pk_bf16(s1[k], s1[k + 1]);
        const unsigned v0 = pk_bf16(bf_lo(u0) * SCALE, bf_hi(u0) * SCALE), v1 = pk_bf16(bf_lo(u1) * SCALE, bf_hi(u1) * SCALE);
        sc[k] = bf_lo(v0) * LOG2E; sc[k + 1] = bf_hi(v0) * LOG2E; sc[4 + k] = bf_lo(v1) * LOG2E; sc[4 + k + 1] = bf_hi(v1) * LOG2E;
      }
      float pm = fmaxf(fmaxf(fmaxf(sc[0], sc[1]), fmaxf(sc[2], sc[3])), fmaxf(fmaxf(sc[4], sc[5]), fmaxf(sc[6], sc[7])));
      if (__any(pm > mrun)) {
        pm = x32_max(x16_max(pm));
        const float mn = fmaxf(mrun, pm);
        const float alpha = __builtin_amdgcn_exp2f(mrun - mn);
        mrun = mn;
        lsum *= alpha;
#pragma unroll
        for (int d = 0; d < 8; ++d) O[d] *= alpha;
      }
      float pr[8];
#pragma unroll
      for (int k = 0; k < 8; ++k) pr[k] = __builtin_amdgcn_exp2f(sc[k] - mrun);
      lsum += ((pr[0] + pr[1]) + (pr[2] + pr[3])) + ((pr[4] + pr[5]) + (pr[6] + pr[7]));
      const v4u pf = {pk_bf16(pr[0], pr[1]), pk_bf16(pr[2], pr[3]), pk_bf16(pr[4], pr[5]), pk_bf16(pr[6], pr[7])};
#pragma unroll
      for (int d = 0; d < 8; ++d) O[d] = mfma16(vf[d], pf, O[d]);
    }
    if (dead) { C[C_ABORT] = 1; return; }
    if (cw == 0) STAMP(3);
    lsum = x32_sum(x16_sum(lsum));
    LAS float* ML = (LAS float*)(L + L_ML);
    LAS float* OB = (LAS float*)(L + L_O);
    if (r16 < 4) {
#pragma unroll
      for (int d = 0; d < 8; ++d) *(LAS f32x4*)(OB + (cw * 4 + r16) * 128 + 16 * d + 4 * g4) = O[d];
      if (g4 == 0) *(LAS f32x2*)(ML + (cw * 4 + r16) * 2) = f32x2{mrun, lsum};
    }
    WAIT_LGKM0();
    if (lane == 0) __hip_atomic_fetch_add((LAS int*)&C[C_MCNT], 1, __ATOMIC_RELAXED, __HIP_MEMORY_SCOPE_WORKGROUP);
    {
      bool ok; SPIN(rfl(C[C_MCNT]) >= NC, 0, ok);
      if (!ok) { C[C_ABORT] = 1; return; }
    }
    {
      const int d0 = (lane >> 4) * 32 + ((lane >> 3) & 1) * 16 + (lane & 7);
      float mw[NC], lw[NC], o0[NC], o1[NC];
#pragma unroll
      for (int w = 0; w < NC; ++w) {
        const f32x2 ml = *(LAS f32x2*)(ML + (w * 4 + cw) * 2);
        mw[w] = ml[0]; lw[w] = ml[1];
        o0[w] = OB[(w * 4 + cw) * 128 + d0]; o1[w] = OB[(w * 4 + cw) * 128 + d0 + 8];
      }
      const float M = fmaxf(fmaxf(mw[0], mw[1]), fmaxf(mw[2], mw[3]));
      float Ls = 0.f, a0 = 0.f, a1 = 0.f;
#pragma unroll
      for (int w = 0; w < NC; ++w) { const float wt = __builtin_amdgcn_exp2f(mw[w] - M); Ls = fmaf(wt, lw[w], Ls); a0 = fmaf(wt, o0[w], a0); a1 = fmaf(wt, o1[w], a1); }
      const float inv = 1.f / Ls;
      const int h = 4 * G + cw;
      const v4u gr = {__float_as_uint(a0 * inv), tag, __float_as_uint(a1 * inv), tag};
      __builtin_amdgcn_raw_buffer_store_b128(gr, rs, W_P1 + ((h * 4 + (lane >> 4)) * 32 + gi) * 256 + (lane & 15) * 16, 0, AUX_SC1);
      if (lane == 0) {
        const v2u lg = {__float_as_uint(M + __builtin_amdgcn_logf(Ls)), tag};
        __builtin_amdgcn_raw_buffer_store_b64(lg, rs, W_LSE + (h * 32 + (gi & 7) * 4 + (gi >> 3)) * 8, 0, AUX_SC1);
      }
    }
    if (cw == 0) STAMP(4);
    unsigned wp[2][8];
#pragma unroll
    for (int kk = 0; kk < 2; ++kk)
#pragma unroll
      for (int k = 0; k < 4; ++k) {
        wp[kk][2 * k] = __builtin_amdgcn_perm(wov[2 * kk + 1][k], wov[2 * kk][k], 0x05040100u);
        wp[kk][2 * k + 1] = __builtin_amdgcn_perm(wov[2 * kk + 1][k], wov[2 * kk][k], 0x07060302u);
      }
    v4u pd[4][2], pl[2];
    {
      const unsigned dbase = W_P1 + ((hp * 4 + cw) * 32 + p) * 256 + r3 * 16;
      const unsigned lbase = W_LSE + (hp * 32 + p * 4) * 8;
      auto pass = [&]() -> bool {
#pragma unroll
        for (int i = 0; i < 4; ++i) {
          pd[i][0] = __builtin_amdgcn_raw_buffer_load_b128(rs, dbase + i * 2048, 0, AUX_SC1);
          pd[i][1] = __builtin_amdgcn_raw_buffer_load_b128(rs, dbase + i * 2048 + 128, 0, AUX_SC1);
        }
        pl[0] = __builtin_amdgcn_raw_buffer_load_b128(rs, lbase, 0, AUX_SC1);
        pl[1] = __builtin_amdgcn_raw_buffer_load_b128(rs, lbase + 16, 0, AUX_SC1);
        unsigned bad = (pl[0][1] ^ tag) | (pl[0][3] ^ tag) | (pl[1][1] ^ tag) | (pl[1][3] ^ tag);
#pragma unroll
        for (int i = 0; i < 4; ++i) bad |= (pd[i][0][1] ^ tag) | (pd[i][0][3] ^ tag) | (pd[i][1][1] ^ tag) | (pd[i][1][3] ^ tag);
        return __all(bad == 0u);
      };
      bool ok; SPIN(pass(), 4, ok);
      if (!ok) { C[C_ABORT] = 1; return; }
    }
    if (cw == 0) STAMP(5);
    {
      const float e0 = __uint_as_float(pl[0][0]), e1 = __uint_as_float(pl[0][2]), e2 = __uint_as_float(pl[1][0]), e3 = __uint_as_float(pl[1][2]);
      float M = fmaxf(fmaxf(e0, e1), fmaxf(e2, e3));
      M = fmaxf(M, dpp<XOR1>(M)); M = fmaxf(M, dpp<XOR2>(M)); M = fmaxf(M, dpp<XOR7>(M));
      const float wt[4] = {__builtin_amdgcn_exp2f(e0 - M), __builtin_amdgcn_exp2f(e1 - M), __builtin_amdgcn_exp2f(e2 - M), __builtin_amdgcn_exp2f(e3 - M)};
      float Ls = (wt[0] + wt[1]) + (wt[2] + wt[3]);
      float o[4] = {0.f, 0.f, 0.f, 0.f};
#pragma unroll
      for (int i = 0; i < 4; ++i) {
        o[0] = fmaf(wt[i], __uint_as_float(pd[i][0][0]), o[0]); o[1] = fmaf(wt[i], __uint_as_float(pd[i][0][2]), o[1]);
        o[2] = fmaf(wt[i], __uint_as_float(pd[i][1][0]), o[2]); o[3] = fmaf(wt[i], __uint_as_float(pd[i][1][2]), o[3]);
      }
      Ls += dpp<XOR1>(Ls); Ls += dpp<XOR2>(Ls); Ls += dpp<XOR7>(Ls);
#pragma unroll
      for (int k = 0; k < 4; ++k) { o[k] += dpp<XOR1>(o[k]); o[k] += dpp<XOR2>(o[k]); o[k] += dpp<XOR7>(o[k]); }
      const float inv = 1.f / Ls;
      const unsigned op0 = pk_bf16(o[0] * inv, o[1] * inv), op1 = pk_bf16(o[2] * inv, o[3] * inv);
      float y[8];
#pragma unroll
      for (int n = 0; n < 8; ++n) y[n] = dot2(wp[1][n], op1, dot2(wp[0][n], op0, 0.f));
      const bool b3 = lane & 8, b4 = lane & 16, b5 = lane & 32;
#pragma unroll
      for (int k = 0; k < 4; ++k) { const float keep = b3 ? y[k + 4] : y[k], send = b3 ? y[k] : y[k + 4]; y[k] = keep + dpp<XOR8>(send); }
#pragma unroll
      for (int k = 0; k < 2; ++k) {
        auto s = __builtin_amdgcn_permlane16_swap(__float_as_uint(y[k]), __float_as_uint(y[k + 2]), false, false);
        y[k] = __uint_as_float(s[0]) + __uint_as_float(s[1]);
      }
      {
        auto s = __builtin_amdgcn_permlane32_swap(__float_as_uint(y[0]), __float_as_uint(y[1]), false, false);
        y[0] = __uint_as_float(s[0]) + __uint_as_float(s[1]);
      }
      (void)b4; (void)b5;
      const int n = 8 * p + ((lane >> 3) & 1) * 4 + ((lane >> 4) & 1) * 2 + (lane >> 5);
      LAS float* Y = (LAS float*)(L + L_Y);
      Y[cw * 64 + n] = y[0];
      WAIT_LGKM0();
      int old = 0;
      if (lane == 0) old = __hip_atomic_fetch_add((LAS int*)&C[C_YCNT], 1, __ATOMIC_RELAXED, __HIP_MEMORY_SCOPE_WORKGROUP);
      if (rfl(old) == NC - 1) {
        asm volatile("" ::: "memory");
        if (lane < 32) {
          const f32x2 p0 = *(LAS f32x2*)(Y + 2 * lane), p1 = *(LAS f32x2*)(Y + 64 + 2 * lane), p2 = *(LAS f32x2*)(Y + 128 + 2 * lane), p3 = *(LAS f32x2*)(Y + 192 + 2 * lane);
          const v4u gr = {__float_as_uint((p0[0] + p1[0]) + (p2[0] + p3[0])), tag, __float_as_uint((p0[1] + p1[1]) + (p2[1] + p3[1])), tag};
          __builtin_amdgcn_raw_buffer_store_b128(gr, rs, W_P2 + ((jc * 32 + lane) * 32 + hp) * 16, 0, AUX_SC1);
        }
        STAMP(6);
      }
    }
    return;
  }

  const unsigned tag = __hip_atomic_load((GAS unsigned*)(ws + W_EPOCH), __ATOMIC_RELAXED, __HIP_MEMORY_SCOPE_AGENT) + 1u;
  const v4u xr = *(const GAS v4u*)(x + 8 * lane);
  __builtin_amdgcn_s_barrier();
  bool ok;
  {
    v4u g0, g1;
    auto pass = [&]() -> bool {
      g0 = __builtin_amdgcn_raw_buffer_load_b128(rs, W_QG + G * 2048 + lane * 32, 0, AUX_SC1);
      g1 = __builtin_amdgcn_raw_buffer_load_b128(rs, W_QG + G * 2048 + lane * 32 + 16, 0, AUX_SC1);
      return __all(((g0[1] ^ tag) | (g0[3] ^ tag) | (g1[1] ^ tag) | (g1[3] ^ tag)) == 0u);
    };
    SPIN(pass(), 2, ok);
    if (!ok) { C[C_ABORT] = 1; return; }
    *(LAS v4u*)(L + L_Q + lane * 16) = v4u{g0[0], g0[2], g1[0], g1[2]};
    WAIT_LGKM0();
    C[C_QRDY] = 1;
    STAMP(7);
  }
  float gate0, gate1, x0, x1;
  {
    float xv[8];
#pragma unroll
    for (int k = 0; k < 4; ++k) { xv[2 * k] = bf_lo(xr[k]); xv[2 * k + 1] = bf_hi(xr[k]); }
    float m, den, m2, den2;
    ln_stats(xv, m, den);
#pragma unroll
    for (int k = 0; k < 8; ++k) xv[k] = ln_apply(xv[k], m, den);
    ln_stats(xv, m2, den2);
    const unsigned xd = *(const GAS unsigned*)(x + 2 * b);
    x0 = bf_lo(xd); x1 = bf_hi(xd);
    const float h0 = ln_apply(ln_apply(x0, m, den), m2, den2), h1 = ln_apply(ln_apply(x1, m, den), m2, den2);
    float a0 = 0.f, a1 = 0.f;
    auto gelu = [](float u) {
      const float z2 = -2.f * 0.7978845608028654f * 1.4426950408889634f * fmaf(0.044715f * u * u, u, u);
      return rbf(u * __builtin_amdgcn_rcpf(1.f + __builtin_amdgcn_exp2f(z2)));
    };
#pragma unroll
    for (int i = 0; i < 7; ++i) {
      const int f = 4 * (lane + 64 * i);
      const v2u a = *(const GAS v2u*)(w1 + f), bb = *(const GAS v2u*)(b1 + f), cc = *(const GAS v2u*)(w2 + f);
#pragma unroll
      for (int k = 0; k < 2; ++k) {
        a0 = fmaf(gelu(rbf(rbf(h0 * bf_lo(a[k])) + bf_lo(bb[k]))), bf_lo(cc[k]), a0);
        a0 = fmaf(gelu(rbf(rbf(h0 * bf_hi(a[k])) + bf_hi(bb[k]))), bf_hi(cc[k]), a0);
        a1 = fmaf(gelu(rbf(rbf(h1 * bf_lo(a[k])) + bf_lo(bb[k]))), bf_lo(cc[k]), a1);
        a1 = fmaf(gelu(rbf(rbf(h1 * bf_hi(a[k])) + bf_hi(bb[k]))), bf_hi(cc[k]), a1);
      }
    }
    const float bias2 = bf_lo((unsigned)b2[0]);
    const float y0 = rbf(rbf(wave_sum(a0)) + bias2), y1 = rbf(rbf(wave_sum(a1)) + bias2);
    gate0 = rbf(y0 / (1.f + __expf(-y0)));
    gate1 = rbf(y1 / (1.f + __expf(-y1)));
  }
  STAMP(8);
  SPIN(rfl(C[C_MCNT]) >= NC, 8, ok);
  if (!ok) return;
  {
    v4u g;
    auto pass = [&]() -> bool {
      g = __builtin_amdgcn_raw_buffer_load_b128(rs, W_P2 + (b * 32 + (lane & 31)) * 16, 0, AUX_SC1);
      return __all(((g[1] ^ tag) | (g[3] ^ tag)) == 0u);
    };
    SPIN(pass(), 2, ok);
    if (!ok) return;
    float s0 = __uint_as_float(g[0]), s1 = __uint_as_float(g[2]);
    s0 += dpp<XOR1>(s0); s0 += dpp<XOR2>(s0); s0 += dpp<XOR7>(s0); s0 += dpp<XOR8>(s0); s0 = x16_sum(s0);
    s1 += dpp<XOR1>(s1); s1 += dpp<XOR2>(s1); s1 += dpp<XOR7>(s1); s1 += dpp<XOR8>(s1); s1 = x16_sum(s1);
    if (lane == 0) {
      *(GAS unsigned*)(out + 2 * b) = pk_bf16(x0 + rbf(rbf(s0) * gate0), x1 + rbf(rbf(s1) * gate1));
      if (b == 0) __hip_atomic_store((GAS unsigned*)(ws + W_EPOCH), tag, __ATOMIC_RELAXED, __HIP_MEMORY_SCOPE_AGENT);
    }
    STAMP(9);
  }
}
}

extern "C" void kernel_launch(void* const* d_in, const int* in_sizes, int n_in, void* d_out, int out_size, void* d_ws, size_t ws_size,
                              hipStream_t stream) {
  (void)hipFuncSetAttribute((const void*)fused_decode, hipFuncAttributeMaxDynamicSharedMemorySize, LDS_BYTES);
  hipLaunchKernelGGL(fused_decode, dim3(NWG), dim3(NTHR), LDS_BYTES, stream, (const u16*)d_in[0], (const u16*)d_in[1], (const u16*)d_in[2],
                     (const u16*)d_in[3], (const u16*)d_in[4], (const u16*)d_in[6], (const u16*)d_in[7], (const u16*)d_in[8],
                     (const u16*)d_in[9], (u16*)d_out, (unsigned char*)d_ws);
}
```

```cpp
#include <hip/hip_runtime.h>
#include <cstdint>

#define GAS __attribute__((address_space(1)))
#define LAS __attribute__((address_space(3)))
typedef unsigned short u16;
typedef unsigned v4u __attribute__((ext_vector_type(4)));
typedef unsigned v2u __attribute__((ext_vector_type(2)));
typedef float f32x4 __attribute__((ext_vector_type(4)));
typedef float f32x2 __attribute__((ext_vector_type(2)));
typedef short s16x4 __attribute__((ext_vector_type(4)));
typedef __bf16 bf16x8 __attribute__((ext_vector_type(8)));
typedef __bf16 bf16x2 __attribute__((ext_vector_type(2)));

namespace {
constexpr int D = 512, HQ = 32, HKV = 8, SK = 65536, DH = 128, FF = 1792;
constexpr int NWG = 256, NC = 4, NWAVE = NC + 2, NTHR = NWAVE * 64;
constexpr int TK = 32;
constexpr int SLOT = 2 * TK * 256;
constexpr int NSLOT = 9;
constexpr int TPW = SK / 32 / TK;
constexpr int L_RING = 0;
constexpr int L_CTRL = NSLOT * SLOT;
constexpr int C_FULL = 0, C_TOTAL = 1, C_QRDY = 2, C_QCNT = 3, C_MCNT = 4, C_YCNT = 5, C_ABORT = 6, C_FREE = 16;
constexpr int L_Q = L_CTRL + 256;
constexpr int L_QP = L_Q + 1024;
constexpr int L_ML = L_QP + 256;
constexpr int L_Y = L_ML + 128;
constexpr int L_O = L_Y + 1024;
constexpr int LDS_BYTES = L_O + 8192;
static_assert(LDS_BYTES <= 160 * 1024, "LDS");
constexpr unsigned W_EPOCH = 0;
constexpr unsigned W_QG = 4096;
constexpr unsigned W_LSE = W_QG + 8 * 256 * 8;
constexpr unsigned W_P2 = W_LSE + 32 * 32 * 8;
constexpr unsigned W_P1 = W_P2 + 256 * 64 * 8;
constexpr unsigned W_END = W_P1 + 32 * 4 * 32 * 32 * 8;
constexpr unsigned W_STAMP = W_END;
constexpr int AUX_SC1 = 16, AUX_NT = 2;
constexpr unsigned SPIN_CAP = 1u << 22;

__device__ __forceinline__ float bf_lo(unsigned u) { return __uint_as_float(u << 16); }
__device__ __forceinline__ float bf_hi(unsigned u) { return __uint_as_float(u & 0xffff0000u); }
__device__ __forceinline__ unsigned pk_bf16(float lo, float hi) {
  bf16x2 r = {(__bf16)lo, (__bf16)hi};
  return __builtin_bit_cast(unsigned, r);
}
__device__ __forceinline__ float rbf(float f) { return bf_lo(pk_bf16(f, f)); }
template <int CTRL> __device__ __forceinline__ float dpp(float x) {
  return __builtin_bit_cast(float, __builtin_amdgcn_mov_dpp(__builtin_bit_cast(int, x), CTRL, 0xf, 0xf, true));
}
constexpr int XOR1 = 0xB1, XOR2 = 0x4E, XOR7 = 0x141, XOR8 = 0x128;
__device__ __forceinline__ float x16_sum(float x) { auto s = __builtin_amdgcn_permlane16_swap(__float_as_uint(x), __float_as_uint(x), false, false); return __uint_as_float(s[0]) + __uint_as_float(s[1]); }
__device__ __forceinline__ float x32_sum(float x) { auto s = __builtin_amdgcn_permlane32_swap(__float_as_uint(x), __float_as_uint(x), false, false); return __uint_as_float(s[0]) + __uint_as_float(s[1]); }
__device__ __forceinline__ float x16_max(float x) { auto s = __builtin_amdgcn_permlane16_swap(__float_as_uint(x), __float_as_uint(x), false, false); return fmaxf(__uint_as_float(s[0]), __uint_as_float(s[1])); }
__device__ __forceinline__ float x32_max(float x) { auto s = __builtin_amdgcn_permlane32_swap(__float_as_uint(x), __float_as_uint(x), false, false); return fmaxf(__uint_as_float(s[0]), __uint_as_float(s[1])); }
__device__ __forceinline__ float wave_sum(float v) {
  v += dpp<XOR1>(v); v += dpp<XOR2>(v); v += dpp<XOR7>(v); v += dpp<XOR8>(v);
  return x32_sum(x16_sum(v));
}
__device__ __forceinline__ float dot2(unsigned a, unsigned b, float acc) {
  return __builtin_amdgcn_fdot2_f32_bf16(__builtin_bit_cast(bf16x2, a), __builtin_bit_cast(bf16x2, b), acc, false);
}
__device__ __forceinline__ f32x4 mfma16(v4u a, v4u b, f32x4 c) {
  return __builtin_amdgcn_mfma_f32_16x16x32_bf16(__builtin_bit_cast(bf16x8, a), __builtin_bit_cast(bf16x8, b), c, 0, 0, 0);
}
#define VPOL 0
#define GLDS(src, dst, off) __builtin_amdgcn_global_load_lds((const GAS unsigned*)(src), (LAS unsigned*)(dst), 16, (off), AUX_NT)
#define GLDV(src, dst, off) __builtin_amdgcn_global_load_lds((const GAS unsigned*)(src), (LAS unsigned*)(dst), 16, (off), VPOL)
#define WAIT_VM(n) asm volatile("s_waitcnt vmcnt(" #n ")" ::: "memory")
#define WAIT_LGKM0() asm volatile("s_waitcnt lgkmcnt(0)" ::: "memory")
__device__ __forceinline__ int rfl(int v) { return __builtin_amdgcn_readfirstlane(v); }

__device__ __forceinline__ void ln_stats(const float (&v)[8], float& m, float& den) {
  float s = 0.f;
#pragma unroll
  for (int k = 0; k < 8; ++k) s += v[k];
  const float mean = wave_sum(s) * (1.f / D);
  float q = 0.f;
#pragma unroll
  for (int k = 0; k < 8; ++k) { const float d = v[k] - mean; q = fmaf(d, d, q); }
  const float var = wave_sum(q) * (1.f / D);
  m = rbf(mean);
  den = rbf(sqrtf(rbf(rbf(var) + 1e-5f)));
}
__device__ __forceinline__ float ln_apply(float x, float m, float den) { return rbf(rbf(x - m) / den); }

#define SPIN(cond, sleepn, okvar) do { unsigned sp_ = 0; okvar = true; _Pragma("nounroll") for (;;) { asm volatile("" ::: "memory"); if (cond) break; \
    if (sleepn) __builtin_amdgcn_s_sleep(sleepn); \
    if ((++sp_ & 63u) == 0u && (sp_ > SPIN_CAP || rfl(C[C_ABORT]) != 0)) { okvar = false; break; } } asm volatile("" ::: "memory"); } while (0)

#define STAMP(i) do {} while (0)

__global__ void __launch_bounds__(NTHR, 2) fused_decode(const u16* __restrict__ x, const u16* __restrict__ kc, const u16* __restrict__ vc,
                                                         const u16* __restrict__ wq, const u16* __restrict__ wo, const u16* __restrict__ w1,
                                                         const u16* __restrict__ b1, const u16* __restrict__ w2, const u16* __restrict__ b2,
                                                         u16* __restrict__ out, unsigned char* __restrict__ ws) {
  extern __shared__ __attribute__((aligned(16))) unsigned char smem[];
  LAS unsigned char* L = (LAS unsigned char*)smem;
  volatile LAS int* C = (volatile LAS int*)(L + L_CTRL);
  const int tid = threadIdx.x, lane = tid & 63, wave = rfl(tid >> 6), b = blockIdx.x;
  const int G = b >> 5, gi = b & 31;
  const __amdgpu_buffer_rsrc_t rs = __builtin_amdgcn_make_buffer_rsrc((void*)ws, 0, (int)W_END, 0x00020000);

  if (__builtin_expect(wave == 0, 1)) {
    STAMP(0);
    const GAS unsigned char* kb = (const GAS unsigned char*)kc + ((size_t)G * SK + (size_t)gi * (TPW * TK)) * 256;
    const GAS unsigned char* vb = (const GAS unsigned char*)vc + ((size_t)G * SK + (size_t)gi * (TPW * TK)) * 256;
    const int r4 = lane >> 4, c = lane & 15;
    unsigned ko[4], vo[2];
#pragma unroll
    for (int j = 0; j < 4; ++j) ko[j] = r4 * 256 + ((c ^ (4 * j + r4)) * 16);
#pragma unroll
    for (int j = 0; j < 2; ++j) vo[j] = r4 * 256 + ((c ^ ((4 * j + r4) << 1)) * 16);
    auto issue = [&](int t, int s) {
      const GAS unsigned char* ks = kb + (size_t)t * (TK * 256);
      const GAS unsigned char* vs = vb + (size_t)t * (TK * 256);
      LAS unsigned char* dst = L + s * SLOT;
      GLDS(ks + ko[0], dst, 0); GLDS(ks + ko[1], dst, 1024); GLDS(ks + ko[2], dst, 2048); GLDS(ks + ko[3], dst, 3072);
      GLDS(ks + 4096 + ko[0], dst + 4096, 0); GLDS(ks + 4096 + ko[1], dst + 4096, 1024); GLDS(ks + 4096 + ko[2], dst + 4096, 2048); GLDS(ks + 4096 + ko[3], dst + 4096, 3072);
      GLDV(vs + vo[0], dst + 8192, 0); GLDV(vs + vo[1], dst + 8192, 1024); GLDV(vs + vo[0], dst + 8192, 2048); GLDV(vs + vo[1], dst + 8192, 3072);
      GLDV(vs + 4096 + vo[0], dst + 12288, 0); GLDV(vs + 4096 + vo[1], dst + 12288, 1024); GLDV(vs + 4096 + vo[0], dst + 12288, 2048); GLDV(vs + 4096 + vo[1], dst + 12288, 3072);
    };
    __builtin_amdgcn_s_barrier();
    issue(0, 0);
    issue(1, 1);
    issue(2, 2);
    WAIT_VM(32); C[C_FULL] = 1;
    int pub = 1, s = 3;
    bool dead = false;
    for (int t = 3; t < TPW; ++t) {
      if (t >= NSLOT) {
        const int need = t - NSLOT + 1;
        if (rfl(C[C_FREE + s]) < need) {
          if (pub < t - 1) { WAIT_VM(16); pub = t - 1; C[C_FULL] = pub; }
          WAIT_VM(0); pub = t; C[C_FULL] = pub;
          bool ok; SPIN(rfl(C[C_FREE + s]) >= need, 1, ok);
          if (!ok) { dead = true; break; }
        }
        asm volatile("" ::: "memory");
      }
      issue(t, s);
      if (pub < t - 1) { WAIT_VM(32); pub = t - 1; C[C_FULL] = pub; }
      s = (s + 1 == NSLOT) ? 0 : s + 1;
    }
    if (dead) { WAIT_VM(0); C[C_ABORT] = 1; return; }
    WAIT_VM(16); C[C_FULL] = TPW - 1;
    WAIT_VM(0); C[C_FULL] = TPW;
    STAMP(1);
    return;
  }

  if (wave <= NC) {
    const int cw = wave - 1;
    const int jrow = (lane & 3) | ((lane >> 3) << 2), half = (lane >> 2) & 1;
    const int col0 = G * 512 + ((gi & 7) * 4 + (gi >> 3)) * 16;
    v4u wv[4];
#pragma unroll
    for (int i = 0; i < 4; ++i) wv[i] = *(const GAS v4u*)(wq + (size_t)(16 * jrow + 4 * cw + i) * 4096 + col0 + 8 * half);
    const v2u xq = *(const GAS v2u*)(x + 16 * jrow + 4 * cw);
    const v4u xr = *(const GAS v4u*)(x + 8 * lane);
    const unsigned tag = __hip_atomic_load((GAS unsigned*)(ws + W_EPOCH), __ATOMIC_RELAXED, __HIP_MEMORY_SCOPE_AGENT) + 1u;
    if (tid < 64 + 64) { C[lane] = (lane == C_TOTAL) ? TPW : 0; }
    WAIT_LGKM0();
    __builtin_amdgcn_sched_barrier(0);
    __builtin_amdgcn_s_barrier();
    __builtin_amdgcn_sched_barrier(0);
    if (cw == 0) STAMP(10);
    {
      float xv[8];
#pragma unroll
      for (int k = 0; k < 4; ++k) { xv[2 * k] = bf_lo(xr[k]); xv[2 * k + 1] = bf_hi(xr[k]); }
      float m, den;
      ln_stats(xv, m, den);
      float xn[4] = {ln_apply(bf_lo(xq[0]), m, den), ln_apply(bf_hi(xq[0]), m, den), ln_apply(bf_lo(xq[1]), m, den), ln_apply(bf_hi(xq[1]), m, den)};
      float a[8];
#pragma unroll
      for (int k = 0; k < 8; ++k) a[k] = 0.f;
#pragma unroll
      for (int i = 0; i < 4; ++i)
#pragma unroll
        for (int k = 0; k < 4; ++k) { a[2 * k] = fmaf(xn[i], bf_lo(wv[i][k]), a[2 * k]); a[2 * k + 1] = fmaf(xn[i], bf_hi(wv[i][k]), a[2 * k + 1]); }
      const bool b0 = lane & 1, b1_ = lane & 2, b3 = lane & 8;
#pragma unroll
      for (int k = 0; k < 4; ++k) { const float keep = b0 ? a[k + 4] : a[k], send = b0 ? a[k] : a[k + 4]; a[k] = keep + dpp<XOR1>(send); }
#pragma unroll
      for (int k = 0; k < 2; ++k) { const float keep = b1_ ? a[k + 2] : a[k], send = b1_ ? a[k] : a[k + 2]; a[k] = keep + dpp<XOR2>(send); }
      { const float keep = b3 ? a[1] : a[0], send = b3 ? a[0] : a[1]; a[0] = keep + dpp<XOR8>(send); }
      const float qs = x32_sum(x16_sum(a[0]));
      const int qc = half * 8 + (lane & 1) * 4 + ((lane >> 1) & 1) * 2 + ((lane >> 3) & 1);
      LAS float* QP = (LAS float*)(L + L_QP);
      if (lane < 16) QP[cw * 16 + qc] = qs;
      WAIT_LGKM0();
      int old = 0;
      if (lane == 0) old = __hip_atomic_fetch_add((LAS int*)&C[C_QCNT], 1, __ATOMIC_RELAXED, __HIP_MEMORY_SCOPE_WORKGROUP);
      if (rfl(old) == NC - 1) {
        asm volatile("" ::: "memory");
        if (lane < 8) {
          const f32x2 p0 = *(LAS f32x2*)(QP + 2 * lane), p1 = *(LAS f32x2*)(QP + 16 + 2 * lane), p2 = *(LAS f32x2*)(QP + 32 + 2 * lane), p3 = *(LAS f32x2*)(QP + 48 + 2 * lane);
          const unsigned pq = pk_bf16((p0[0] + p1[0]) + (p2[0] + p3[0]), (p0[1] + p1[1]) + (p2[1] + p3[1]));
          __hip_atomic_store((GAS unsigned long long*)(ws + W_QG) + (col0 >> 1) + lane, ((unsigned long long)tag << 32) | pq, __ATOMIC_RELAXED, __HIP_MEMORY_SCOPE_AGENT);
        }
        STAMP(2);
      }
    }
    const int hp = b >> 3, jc = b & 7, r3 = lane >> 3, p = lane & 7;
    v4u wov[4];
#pragma unroll
    for (int k = 0; k < 4; ++k) wov[k] = *(const GAS v4u*)(wo + (size_t)(hp * 128 + 32 * cw + 8 * k + r3) * 512 + 64 * jc + 8 * p);

    const int g4 = lane >> 4, r16 = lane & 15;
    unsigned kaddr[4];
#pragma unroll
    for (int s = 0; s < 4; ++s) kaddr[s] = r16 * 256 + (((4 * s + g4) ^ r16) * 16);
    const int vrow = 4 * g4 + ((lane >> 2) & 3);
    const unsigned vaddr0 = 8192 + vrow * 256 + (vrow & 7) * 32 + (lane & 3) * 8;
    v4u qf[4];
    f32x4 O[8];
#pragma unroll
    for (int d = 0; d < 8; ++d) O[d] = f32x4{0.f, 0.f, 0.f, 0.f};
    float mrun = -INFINITY, lsum = 0.f;
    constexpr float SCALE = 0.08838834764831845f, LOG2E = 1.4426950408889634f;
    bool dead = false;
    int slot = cw;
    for (int t = cw; t < TPW; t += NC) {
      bool ok; SPIN(rfl(C[C_FULL]) > t, 0, ok);
      if (!ok) { dead = true; break; }
      const unsigned sb = slot * SLOT;
      v4u kf[2][4];
      v4u vf[8];
#pragma unroll
      for (int s = 0; s < 4; ++s) {
        kf[0][s] = *(const LAS v4u*)(L + sb + kaddr[s]);
        kf[1][s] = *(const LAS v4u*)(L + sb + kaddr[s] + 4096);
      }
#pragma unroll
      for (int d = 0; d < 8; ++d) {
        const unsigned a = (sb + vaddr0) ^ (d * 32);
        const s16x4 lo = __builtin_amdgcn_ds_read_tr16_b64_v4i16((LAS s16x4*)(L + a));
        const s16x4 hi = __builtin_amdgcn_ds_read_tr16_b64_v4i16((LAS s16x4*)(L + a + 4096));
        const v2u l2 = __builtin_bit_cast(v2u, lo), h2 = __builtin_bit_cast(v2u, hi);
        vf[d] = v4u{l2[0], l2[1], h2[0], h2[1]};
      }
      WAIT_LGKM0();
      C[C_FREE + slot] = t + 1;
      asm volatile("" : "+v"(kf[0][0]), "+v"(kf[0][1]), "+v"(kf[0][2]), "+v"(kf[0][3]), "+v"(kf[1][0]), "+v"(kf[1][1]), "+v"(kf[1][2]), "+v"(kf[1][3]) :: "memory");
      asm volatile("" : "+v"(vf[0]), "+v"(vf[1]), "+v"(vf[2]), "+v"(vf[3]), "+v"(vf[4]), "+v"(vf[5]), "+v"(vf[6]), "+v"(vf[7]) :: "memory");
      slot += NC; if (slot >= NSLOT) slot -= NSLOT;
      if (t == cw) {
        SPIN(rfl(C[C_QRDY]) != 0, 1, ok);
        if (!ok) { dead = true; break; }
#pragma unroll
        for (int s = 0; s < 4; ++s) qf[s] = *(const LAS v4u*)(L + L_Q + (lane & 3) * 256 + s * 64 + g4 * 16);
      }
      f32x4 s0 = {0.f, 0.f, 0.f, 0.f}, s1 = {0.f, 0.f, 0.f, 0.f};
#pragma unroll
      for (int s = 0; s < 4; ++s) { s0 = mfma16(kf[0][s], qf[s], s0); s1 = mfma16(kf[1][s], qf[s], s1); }
      float sc[8];
#pragma unroll
      for (int k = 0; k < 4; k += 2) {
        const unsigned u0 = pk_bf16(s0[k], s0[k + 1]), u1 = pk_bf16(s1[k], s1[k + 1]);
        const unsigned v0 = pk_bf16(bf_lo(u0) * SCALE, bf_hi(u0) * SCALE), v1 = pk_bf16(bf_lo(u1) * SCALE, bf_hi(u1) * SCALE);
        sc[k] = bf_lo(v0) * LOG2E; sc[k + 1] = bf_hi(v0) * LOG2E; sc[4 + k] = bf_lo(v1) * LOG2E; sc[4 + k + 1] = bf_hi(v1) * LOG2E;
      }
      float pm = fmaxf(fmaxf(fmaxf(sc[0], sc[1]), fmaxf(sc[2], sc[3])), fmaxf(fmaxf(sc[4], sc[5]), fmaxf(sc[6], sc[7])));
      if (__any(pm > mrun)) {
        pm = x32_max(x16_max(pm));
        const float mn = fmaxf(mrun, pm);
        const float alpha = __builtin_amdgcn_exp2f(mrun - mn);
        mrun = mn;
        lsum *= alpha;
#pragma unroll
        for (int d = 0; d < 8; ++d) O[d] *= alpha;
      }
      float pr[8];
#pragma unroll
      for (int k = 0; k < 8; ++k) pr[k] = __builtin_amdgcn_exp2f(sc[k] - mrun);
      lsum += ((pr[0] + pr[1]) + (pr[2] + pr[3])) + ((pr[4] + pr[5]) + (pr[6] + pr[7]));
      const v4u pf = {pk_bf16(pr[0], pr[1]), pk_bf16(pr[2], pr[3]), pk_bf16(pr[4], pr[5]), pk_bf16(pr[6], pr[7])};
#pragma unroll
      for (int d = 0; d < 8; ++d) O[d] = mfma16(vf[d], pf, O[d]);
    }
    if (dead) { C[C_ABORT] = 1; return; }
    if (cw == 0) STAMP(3);
    lsum = x32_sum(x16_sum(lsum));
    LAS float* ML = (LAS float*)(L + L_ML);
    LAS float* OB = (LAS float*)(L + L_O);
    if (r16 < 4) {
#pragma unroll
      for (int d = 0; d < 8; ++d) *(LAS f32x4*)(OB + (cw * 4 + r16) * 128 + 16 * d + 4 * g4) = O[d];
      if (g4 == 0) *(LAS f32x2*)(ML + (cw * 4 + r16) * 2) = f32x2{mrun, lsum};
    }
    WAIT_LGKM0();
    if (lane == 0) __hip_atomic_fetch_add((LAS int*)&C[C_MCNT], 1, __ATOMIC_RELAXED, __HIP_MEMORY_SCOPE_WORKGROUP);
    {
      bool ok; SPIN(rfl(C[C_MCNT]) >= NC, 0, ok);
      if (!ok) { C[C_ABORT] = 1; return; }
    }
    {
      const int d0 = (lane >> 4) * 32 + ((lane >> 3) & 1) * 16 + (lane & 7);
      float mw[NC], lw[NC], o0[NC], o1[NC];
#pragma unroll
      for (int w = 0; w < NC; ++w) {
        const f32x2 ml = *(LAS f32x2*)(ML + (w * 4 + cw) * 2);
        mw[w] = ml[0]; lw[w] = ml[1];
        o0[w] = OB[(w * 4 + cw) * 128 + d0]; o1[w] = OB[(w * 4 + cw) * 128 + d0 + 8];
      }
      const float M = fmaxf(fmaxf(mw[0], mw[1]), fmaxf(mw[2], mw[3]));
      float Ls = 0.f, a0 = 0.f, a1 = 0.f;
#pragma unroll
      for (int w = 0; w < NC; ++w) { const float wt = __builtin_amdgcn_exp2f(mw[w] - M); Ls = fmaf(wt, lw[w], Ls); a0 = fmaf(wt, o0[w], a0); a1 = fmaf(wt, o1[w], a1); }
      const float inv = 1.f / Ls;
      const int h = 4 * G + cw;
      const v4u gr = {__float_as_uint(a0 * inv), tag, __float_as_uint(a1 * inv), tag};
      __builtin_amdgcn_raw_buffer_store_b128(gr, rs, W_P1 + ((h * 4 + (lane >> 4)) * 32 + gi) * 256 + (lane & 15) * 16, 0, AUX_SC1);
      if (lane == 0) {
        const v2u lg = {__float_as_uint(M + __builtin_amdgcn_logf(Ls)), tag};
        __builtin_amdgcn_raw_buffer_store_b64(lg, rs, W_LSE + (h * 32 + (gi & 7) * 4 + (gi >> 3)) * 8, 0, AUX_SC1);
      }
    }
    if (cw == 0) STAMP(4);
    unsigned wp[2][8];
#pragma unroll
    for (int kk = 0; kk < 2; ++kk)
#pragma unroll
      for (int k = 0; k < 4; ++k) {
        wp[kk][2 * k] = __builtin_amdgcn_perm(wov[2 * kk + 1][k], wov[2 * kk][k], 0x05040100u);
        wp[kk][2 * k + 1] = __builtin_amdgcn_perm(wov[2 * kk + 1][k], wov[2 * kk][k], 0x07060302u);
      }
    v4u pd[4][2], pl[2];
    {
      const unsigned dbase = W_P1 + ((hp * 4 + cw) * 32 + p) * 256 + r3 * 16;
      const unsigned lbase = W_LSE + (hp * 32 + p * 4) * 8;
      auto pass = [&]() -> bool {
#pragma unroll
        for (int i = 0; i < 4; ++i) {
          pd[i][0] = __builtin_amdgcn_raw_buffer_load_b128(rs, dbase + i * 2048, 0, AUX_SC1);
          pd[i][1] = __builtin_amdgcn_raw_buffer_load_b128(rs, dbase + i * 2048 + 128, 0, AUX_SC1);
        }
        pl[0] = __builtin_amdgcn_raw_buffer_load_b128(rs, lbase, 0, AUX_SC1);
        pl[1] = __builtin_amdgcn_raw_buffer_load_b128(rs, lbase + 16, 0, AUX_SC1);
        unsigned bad = (pl[0][1] ^ tag) | (pl[0][3] ^ tag) | (pl[1][1] ^ tag) | (pl[1][3] ^ tag);
#pragma unroll
        for (int i = 0; i < 4; ++i) bad |= (pd[i][0][1] ^ tag) | (pd[i][0][3] ^ tag) | (pd[i][1][1] ^ tag) | (pd[i][1][3] ^ tag);
        return __all(bad == 0u);
      };
      bool ok; SPIN(pass(), 4, ok);
      if (!ok) { C[C_ABORT] = 1; return; }
    }
    if (cw == 0) STAMP(5);
    {
      const float e0 = __uint_as_float(pl[0][0]), e1 = __uint_as_float(pl[0][2]), e2 = __uint_as_float(pl[1][0]), e3 = __uint_as_float(pl[1][2]);
      float M = fmaxf(fmaxf(e0, e1), fmaxf(e2, e3));
      M = fmaxf(M, dpp<XOR1>(M)); M = fmaxf(M, dpp<XOR2>(M)); M = fmaxf(M, dpp<XOR7>(M));
      const float wt[4] = {__builtin_amdgcn_exp2f(e0 - M), __builtin_amdgcn_exp2f(e1 - M), __builtin_amdgcn_exp2f(e2 - M), __builtin_amdgcn_exp2f(e3 - M)};
      float Ls = (wt[0] + wt[1]) + (wt[2] + wt[3]);
      float o[4] = {0.f, 0.f, 0.f, 0.f};
#pragma unroll
      for (int i = 0; i < 4; ++i) {
        o[0] = fmaf(wt[i], __uint_as_float(pd[i][0][0]), o[0]); o[1] = fmaf(wt[i], __uint_as_float(pd[i][0][2]), o[1]);
        o[2] = fmaf(wt[i], __uint_as_float(pd[i][1][0]), o[2]); o[3] = fmaf(wt[i], __uint_as_float(pd[i][1][2]), o[3]);
      }
      Ls += dpp<XOR1>(Ls); Ls += dpp<XOR2>(Ls); Ls += dpp<XOR7>(Ls);
#pragma unroll
      for (int k = 0; k < 4; ++k) { o[k] += dpp<XOR1>(o[k]); o[k] += dpp<XOR2>(o[k]); o[k] += dpp<XOR7>(o[k]); }
      const float inv = 1.f / Ls;
      const unsigned op0 = pk_bf16(o[0] * inv, o[1] * inv), op1 = pk_bf16(o[2] * inv, o[3] * inv);
      float y[8];
#pragma unroll
      for (int n = 0; n < 8; ++n) y[n] = dot2(wp[1][n], op1, dot2(wp[0][n], op0, 0.f));
      const bool b3 = lane & 8, b4 = lane & 16, b5 = lane & 32;
#pragma unroll
      for (int k = 0; k < 4; ++k) { const float keep = b3 ? y[k + 4] : y[k], send = b3 ? y[k] : y[k + 4]; y[k] = keep + dpp<XOR8>(send); }
#pragma unroll
      for (int k = 0; k < 2; ++k) {
        auto s = __builtin_amdgcn_permlane16_swap(__float_as_uint(y[k]), __float_as_uint(y[k + 2]), false, false);
        y[k] = __uint_as_float(s[0]) + __uint_as_float(s[1]);
      }
      {
        auto s = __builtin_amdgcn_permlane32_swap(__float_as_uint(y[0]), __float_as_uint(y[1]), false, false);
        y[0] = __uint_as_float(s[0]) + __uint_as_float(s[1]);
      }
      (void)b4; (void)b5;
      const int n = 8 * p + ((lane >> 3) & 1) * 4 + ((lane >> 4) & 1) * 2 + (lane >> 5);
      LAS float* Y = (LAS float*)(L + L_Y);
      Y[cw * 64 + n] = y[0];
      WAIT_LGKM0();
      int old = 0;
      if (lane == 0) old = __hip_atomic_fetch_add((LAS int*)&C[C_YCNT], 1, __ATOMIC_RELAXED, __HIP_MEMORY_SCOPE_WORKGROUP);
      if (rfl(old) == NC - 1) {
        asm volatile("" ::: "memory");
        if (lane < 32) {
          const f32x2 p0 = *(LAS f32x2*)(Y + 2 * lane), p1 = *(LAS f32x2*)(Y + 64 + 2 * lane), p2 = *(LAS f32x2*)(Y + 128 + 2 * lane), p3 = *(LAS f32x2*)(Y + 192 + 2 * lane);
          const v4u gr = {__float_as_uint((p0[0] + p1[0]) + (p2[0] + p3[0])), tag, __float_as_uint((p0[1] + p1[1]) + (p2[1] + p3[1])), tag};
          __builtin_amdgcn_raw_buffer_store_b128(gr, rs, W_P2 + ((jc * 32 + lane) * 32 + hp) * 16, 0, AUX_SC1);
        }
        STAMP(6);
      }
    }
    return;
  }

  const unsigned tag = __hip_atomic_load((GAS unsigned*)(ws + W_EPOCH), __ATOMIC_RELAXED, __HIP_MEMORY_SCOPE_AGENT) + 1u;
  const v4u xr = *(const GAS v4u*)(x + 8 * lane);
  __builtin_amdgcn_s_barrier();
  bool ok;
  {
    v4u g0, g1;
    auto pass = [&]() -> bool {
      g0 = __builtin_amdgcn_raw_buffer_load_b128(rs, W_QG + G * 2048 + lane * 32, 0, AUX_SC1);
      g1 = __builtin_amdgcn_raw_buffer_load_b128(rs, W_QG + G * 2048 + lane * 32 + 16, 0, AUX_SC1);
      return __all(((g0[1] ^ tag) | (g0[3] ^ tag) | (g1[1] ^ tag) | (g1[3] ^ tag)) == 0u);
    };
    SPIN(pass(), 2, ok);
    if (!ok) { C[C_ABORT] = 1; return; }
    *(LAS v4u*)(L + L_Q + lane * 16) = v4u{g0[0], g0[2], g1[0], g1[2]};
    WAIT_LGKM0();
    C[C_QRDY] = 1;
    STAMP(7);
  }
  float gate0, gate1, x0, x1;
  {
    float xv[8];
#pragma unroll
    for (int k = 0; k < 4; ++k) { xv[2 * k] = bf_lo(xr[k]); xv[2 * k + 1] = bf_hi(xr[k]); }
    float m, den, m2, den2;
    ln_stats(xv, m, den);
#pragma unroll
    for (int k = 0; k < 8; ++k) xv[k] = ln_apply(xv[k], m, den);
    ln_stats(xv, m2, den2);
    const unsigned xd = *(const GAS unsigned*)(x + 2 * b);
    x0 = bf_lo(xd); x1 = bf_hi(xd);
    const float h0 = ln_apply(ln_apply(x0, m, den), m2, den2), h1 = ln_apply(ln_apply(x1, m, den), m2, den2);
    float a0 = 0.f, a1 = 0.f;
    auto gelu = [](float u) {
      const float z2 = -2.f * 0.7978845608028654f * 1.4426950408889634f * fmaf(0.044715f * u * u, u, u);
      return rbf(u * __builtin_amdgcn_rcpf(1.f + __builtin_amdgcn_exp2f(z2)));
    };
#pragma unroll
    for (int i = 0; i < 7; ++i) {
      const int f = 4 * (lane + 64 * i);
      const v2u a = *(const GAS v2u*)(w1 + f), bb = *(const GAS v2u*)(b1 + f), cc = *(const GAS v2u*)(w2 + f);
#pragma unroll
      for (int k = 0; k < 2; ++k) {
        a0 = fmaf(gelu(rbf(rbf(h0 * bf_lo(a[k])) + bf_lo(bb[k]))), bf_lo(cc[k]), a0);
        a0 = fmaf(gelu(rbf(rbf(h0 * bf_hi(a[k])) + bf_hi(bb[k]))), bf_hi(cc[k]), a0);
        a1 = fmaf(gelu(rbf(rbf(h1 * bf_lo(a[k])) + bf_lo(bb[k]))), bf_lo(cc[k]), a1);
        a1 = fmaf(gelu(rbf(rbf(h1 * bf_hi(a[k])) + bf_hi(bb[k]))), bf_hi(cc[k]), a1);
      }
    }
    const float bias2 = bf_lo((unsigned)b2[0]);
    const float y0 = rbf(rbf(wave_sum(a0)) + bias2), y1 = rbf(rbf(wave_sum(a1)) + bias2);
    gate0 = rbf(y0 / (1.f + __expf(-y0)));
    gate1 = rbf(y1 / (1.f + __expf(-y1)));
  }
  STAMP(8);
  SPIN(rfl(C[C_MCNT]) >= NC, 8, ok);
  if (!ok) return;
  {
    v4u g;
    auto pass = [&]() -> bool {
      g = __builtin_amdgcn_raw_buffer_load_b128(rs, W_P2 + (b * 32 + (lane & 31)) * 16, 0, AUX_SC1);
      return __all(((g[1] ^ tag) | (g[3] ^ tag)) == 0u);
    };
    SPIN(pass(), 2, ok);
    if (!ok) return;
    float s0 = __uint_as_float(g[0]), s1 = __uint_as_float(g[2]);
    s0 += dpp<XOR1>(s0); s0 += dpp<XOR2>(s0); s0 += dpp<XOR7>(s0); s0 += dpp<XOR8>(s0); s0 = x16_sum(s0);
    s1 += dpp<XOR1>(s1); s1 += dpp<XOR2>(s1); s1 += dpp<XOR7>(s1); s1 += dpp<XOR8>(s1); s1 = x16_sum(s1);
    if (lane == 0) {
      *(GAS unsigned*)(out + 2 * b) = pk_bf16(x0 + rbf(rbf(s0) * gate0), x1 + rbf(rbf(s1) * gate1));
      if (b == 0) __hip_atomic_store((GAS unsigned*)(ws + W_EPOCH), tag, __ATOMIC_RELAXED, __HIP_MEMORY_SCOPE_AGENT);
    }
    STAMP(9);
  }
}
}

extern "C" void kernel_launch(void* const* d_in, const int* in_sizes, int n_in, void* d_out, int out_size, void* d_ws, size_t ws_size,
                              hipStream_t stream) {
  (void)hipFuncSetAttribute((const void*)fused_decode, hipFuncAttributeMaxDynamicSharedMemorySize, LDS_BYTES);
  hipLaunchKernelGGL(fused_decode, dim3(NWG), dim3(NTHR), LDS_BYTES, stream, (const u16*)d_in[0], (const u16*)d_in[1], (const u16*)d_in[2],
                     (const u16*)d_in[3], (const u16*)d_in[4], (const u16*)d_in[6], (const u16*)d_in[7], (const u16*)d_in[8],
                     (const u16*)d_in[9], (u16*)d_out, (unsigned char*)d_ws);
}
```

```cpp
#include <hip/hip_runtime.h>
#include <cstdint>
#include <cstdio>

#define GAS __attribute__((address_space(1)))
#define LAS __attribute__((address_space(3)))
typedef unsigned short u16;
typedef unsigned v4u __attribute__((ext_vector_type(4)));
typedef unsigned v2u __attribute__((ext_vector_type(2)));
typedef float f32x4 __attribute__((ext_vector_type(4)));
typedef float f32x2 __attribute__((ext_vector_type(2)));
typedef short s16x4 __attribute__((ext_vector_type(4)));
typedef __bf16 bf16x8 __attribute__((ext_vector_type(8)));
typedef __bf16 bf16x2 __attribute__((ext_vector_type(2)));

namespace {
constexpr int D = 512, HQ = 32, HKV = 8, SK = 65536, DH = 128, FF = 1792;
constexpr int NWG = 256, NC = 4, NWAVE = NC + 2, NTHR = NWAVE * 64;
constexpr int TK = 32;
constexpr int SLOT = 2 * TK * 256;
constexpr int NSLOT = 9;
constexpr int TPW = SK / 32 / TK;
constexpr int L_RING = 0;
constexpr int L_CTRL = NSLOT * SLOT;
constexpr int C_FULL = 0, C_TOTAL = 1, C_QRDY = 2, C_QCNT = 3, C_MCNT = 4, C_YCNT = 5, C_ABORT = 6, C_FREE = 16;
constexpr int L_Q = L_CTRL + 256;
constexpr int L_QP = L_Q + 1024;
constexpr int L_ML = L_QP + 256;
constexpr int L_Y = L_ML + 128;
constexpr int L_O = L_Y + 1024;
constexpr int LDS_BYTES = L_O + 8192;
static_assert(LDS_BYTES <= 160 * 1024, "LDS");
constexpr unsigned W_EPOCH = 0;
constexpr unsigned W_QG = 4096;
constexpr unsigned W_LSE = W_QG + 8 * 256 * 8;
constexpr unsigned W_P2 = W_LSE + 32 * 32 * 8;
constexpr unsigned W_P1 = W_P2 + 256 * 64 * 8;
constexpr unsigned W_END = W_P1 + 32 * 4 * 32 * 32 * 8;
constexpr unsigned W_STAMP = W_END;
constexpr int AUX_SC1 = 16, AUX_NT = 2;
constexpr unsigned SPIN_CAP = 1u << 22;

__device__ __forceinline__ float bf_lo(unsigned u) { return __uint_as_float(u << 16); }
__device__ __forceinline__ float bf_hi(unsigned u) { return __uint_as_float(u & 0xffff0000u); }
__device__ __forceinline__ unsigned pk_bf16(float lo, float hi) {
  bf16x2 r = {(__bf16)lo, (__bf16)hi};
  return __builtin_bit_cast(unsigned, r);
}
__device__ __forceinline__ float rbf(float f) { return bf_lo(pk_bf16(f, f)); }
template <int CTRL> __device__ __forceinline__ float dpp(float x) {
  return __builtin_bit_cast(float, __builtin_amdgcn_mov_dpp(__builtin_bit_cast(int, x), CTRL, 0xf, 0xf, true));
}
constexpr int XOR1 = 0xB1, XOR2 = 0x4E, XOR7 = 0x141, XOR8 = 0x128;
__device__ __forceinline__ float x16_sum(float x) { auto s = __builtin_amdgcn_permlane16_swap(__float_as_uint(x), __float_as_uint(x), false, false); return __uint_as_float(s[0]) + __uint_as_float(s[1]); }
__device__ __forceinline__ float x32_sum(float x) { auto s = __builtin_amdgcn_permlane32_swap(__float_as_uint(x), __float_as_uint(x), false, false); return __uint_as_float(s[0]) + __uint_as_float(s[1]); }
__device__ __forceinline__ float x16_max(float x) { auto s = __builtin_amdgcn_permlane16_swap(__float_as_uint(x), __float_as_uint(x), false, false); return fmaxf(__uint_as_float(s[0]), __uint_as_float(s[1])); }
__device__ __forceinline__ float x32_max(float x) { auto s = __builtin_amdgcn_permlane32_swap(__float_as_uint(x), __float_as_uint(x), false, false); return fmaxf(__uint_as_float(s[0]), __uint_as_float(s[1])); }
__device__ __forceinline__ float wave_sum(float v) {
  v += dpp<XOR1>(v); v += dpp<XOR2>(v); v += dpp<XOR7>(v); v += dpp<XOR8>(v);
  return x32_sum(x16_sum(v));
}
__device__ __forceinline__ float dot2(unsigned a, unsigned b, float acc) {
  return __builtin_amdgcn_fdot2_f32_bf16(__builtin_bit_cast(bf16x2, a), __builtin_bit_cast(bf16x2, b), acc, false);
}
__device__ __forceinline__ f32x4 mfma16(v4u a, v4u b, f32x4 c) {
  return __builtin_amdgcn_mfma_f32_16x16x32_bf16(__builtin_bit_cast(bf16x8, a), __builtin_bit_cast(bf16x8, b), c, 0, 0, 0);
}
#define VPOL 16
#define GLDS(src, dst, off) __builtin_amdgcn_global_load_lds((const GAS unsigned*)(src), (LAS unsigned*)(dst), 16, (off), AUX_NT)
#define GLDV(src, dst, off) __builtin_amdgcn_global_load_lds((const GAS unsigned*)(src), (LAS unsigned*)(dst), 16, (off), VPOL)
#define WAIT_VM(n) asm volatile("s_waitcnt vmcnt(" #n ")" ::: "memory")
#define WAIT_LGKM0() asm volatile("s_waitcnt lgkmcnt(0)" ::: "memory")
__device__ __forceinline__ int rfl(int v) { return __builtin_amdgcn_readfirstlane(v); }

__device__ __forceinline__ void ln_stats(const float (&v)[8], float& m, float& den) {
  float s = 0.f;
#pragma unroll
  for (int k = 0; k < 8; ++k) s += v[k];
  const float mean = wave_sum(s) * (1.f / D);
  float q = 0.f;
#pragma unroll
  for (int k = 0; k < 8; ++k) { const float d = v[k] - mean; q = fmaf(d, d, q); }
  const float var = wave_sum(q) * (1.f / D);
  m = rbf(mean);
  den = rbf(sqrtf(rbf(rbf(var) + 1e-5f)));
}
__device__ __forceinline__ float ln_apply(float x, float m, float den) { return rbf(rbf(x - m) / den); }

#define SPIN(cond, sleepn, okvar) do { unsigned sp_ = 0; okvar = true; _Pragma("nounroll") for (;;) { asm volatile("" ::: "memory"); if (cond) break; \
    if (sleepn) __builtin_amdgcn_s_sleep(sleepn); \
    if ((++sp_ & 63u) == 0u && (sp_ > SPIN_CAP || rfl(C[C_ABORT]) != 0)) { okvar = false; break; } } asm volatile("" ::: "memory"); } while (0)

#define STAMP(i) do {} while (0)

__global__ void __launch_bounds__(NTHR, 2) fused_decode(const u16* __restrict__ x, const u16* __restrict__ kc, const u16* __restrict__ vc,
                                                         const u16* __restrict__ wq, const u16* __restrict__ wo, const u16* __restrict__ w1,
                                                         const u16* __restrict__ b1, const u16* __restrict__ w2, const u16* __restrict__ b2,
                                                         u16* __restrict__ out, unsigned char* __restrict__ ws) {
  extern __shared__ __attribute__((aligned(16))) unsigned char smem[];
  LAS unsigned char* L = (LAS unsigned char*)smem;
  volatile LAS int* C = (volatile LAS int*)(L + L_CTRL);
  const int tid = threadIdx.x, lane = tid & 63, wave = rfl(tid >> 6), b = blockIdx.x;
  const int G = b >> 5, gi = b & 31;
  const __amdgpu_buffer_rsrc_t rs = __builtin_amdgcn_make_buffer_rsrc((void*)ws, 0, (int)W_END, 0x00020000);

  if (__builtin_expect(wave == 0, 1)) {
    STAMP(0);
    const GAS unsigned char* kb = (const GAS unsigned char*)kc + ((size_t)G * SK + (size_t)gi * (TPW * TK)) * 256;
    const GAS unsigned char* vb = (const GAS unsigned char*)vc + ((size_t)G * SK + (size_t)gi * (TPW * TK)) * 256;
    const int r4 = lane >> 4, c = lane & 15;
    unsigned ko[4], vo[2];
#pragma unroll
    for (int j = 0; j < 4; ++j) ko[j] = r4 * 256 + ((c ^ (4 * j + r4)) * 16);
#pragma unroll
    for (int j = 0; j < 2; ++j) vo[j] = r4 * 256 + ((c ^ ((4 * j + r4) << 1)) * 16);
    auto issue = [&](int t, int s) {
      const GAS unsigned char* ks = kb + (size_t)t * (TK * 256);
      const GAS unsigned char* vs = vb + (size_t)t * (TK * 256);
      LAS unsigned char* dst = L + s * SLOT;
      GLDS(ks + ko[0], dst, 0); GLDS(ks + ko[1], dst, 1024); GLDS(ks + ko[2], dst, 2048); GLDS(ks + ko[3], dst, 3072);
      GLDS(ks + 4096 + ko[0], dst + 4096, 0); GLDS(ks + 4096 + ko[1], dst + 4096, 1024); GLDS(ks + 4096 + ko[2], dst + 4096, 2048); GLDS(ks + 4096 + ko[3], dst + 4096, 3072);
      GLDV(vs + vo[0], dst + 8192, 0); GLDV(vs + vo[1], dst + 8192, 1024); GLDV(vs + vo[0], dst + 8192, 2048); GLDV(vs + vo[1], dst + 8192, 3072);
      GLDV(vs + 4096 + vo[0], dst + 12288, 0); GLDV(vs + 4096 + vo[1], dst + 12288, 1024); GLDV(vs + 4096 + vo[0], dst + 12288, 2048); GLDV(vs + 4096 + vo[1], dst + 12288, 3072);
    };
    __builtin_amdgcn_s_barrier();
    issue(0, 0);
    issue(1, 1);
    issue(2, 2);
    WAIT_VM(32); C[C_FULL] = 1;
    int pub = 1, s = 3;
    bool dead = false;
    for (int t = 3; t < TPW; ++t) {
      if (t >= NSLOT) {
        const int need = t - NSLOT + 1;
        if (rfl(C[C_FREE + s]) < need) {
          if (pub < t - 1) { WAIT_VM(16); pub = t - 1; C[C_FULL] = pub; }
          WAIT_VM(0); pub = t; C[C_FULL] = pub;
          bool ok; SPIN(rfl(C[C_FREE + s]) >= need, 1, ok);
          if (!ok) { dead = true; break; }
        }
        asm volatile("" ::: "memory");
      }
      issue(t, s);
      if (pub < t - 1) { WAIT_VM(32); pub = t - 1; C[C_FULL] = pub; }
      s = (s + 1 == NSLOT) ? 0 : s + 1;
    }
    if (dead) { WAIT_VM(0); C[C_ABORT] = 1; return; }
    WAIT_VM(16); C[C_FULL] = TPW - 1;
    WAIT_VM(0); C[C_FULL] = TPW;
    STAMP(1);
    return;
  }

  if (wave <= NC) {
    const int cw = wave - 1;
    const int jrow = (lane & 3) | ((lane >> 3) << 2), half = (lane >> 2) & 1;
    const int col0 = G * 512 + ((gi & 7) * 4 + (gi >> 3)) * 16;
    v4u wv[4];
#pragma unroll
    for (int i = 0; i < 4; ++i) wv[i] = *(const GAS v4u*)(wq + (size_t)(16 * jrow + 4 * cw + i) * 4096 + col0 + 8 * half);
    const v2u xq = *(const GAS v2u*)(x + 16 * jrow + 4 * cw);
    const v4u xr = *(const GAS v4u*)(x + 8 * lane);
    const unsigned tag = __hip_atomic_load((GAS unsigned*)(ws + W_EPOCH), __ATOMIC_RELAXED, __HIP_MEMORY_SCOPE_AGENT) + 1u;
    if (tid < 64 + 64) { C[lane] = (lane == C_TOTAL) ? TPW : 0; }
    WAIT_LGKM0();
    __builtin_amdgcn_sched_barrier(0);
    __builtin_amdgcn_s_barrier();
    __builtin_amdgcn_sched_barrier(0);
    if (cw == 0) STAMP(10);
    {
      float xv[8];
#pragma unroll
      for (int k = 0; k < 4; ++k) { xv[2 * k] = bf_lo(xr[k]); xv[2 * k + 1] = bf_hi(xr[k]); }
      float m, den;
      ln_stats(xv, m, den);
      float xn[4] = {ln_apply(bf_lo(xq[0]), m, den), ln_apply(bf_hi(xq[0]), m, den), ln_apply(bf_lo(xq[1]), m, den), ln_apply(bf_hi(xq[1]), m, den)};
      float a[8];
#pragma unroll
      for (int k = 0; k < 8; ++k) a[k] = 0.f;
#pragma unroll
      for (int i = 0; i < 4; ++i)
#pragma unroll
        for (int k = 0; k < 4; ++k) { a[2 * k] = fmaf(xn[i], bf_lo(wv[i][k]), a[2 * k]); a[2 * k + 1] = fmaf(xn[i], bf_hi(wv[i][k]), a[2 * k + 1]); }
      const bool b0 = lane & 1, b1_ = lane & 2, b3 = lane & 8;
#pragma unroll
      for (int k = 0; k < 4; ++k) { const float keep = b0 ? a[k + 4] : a[k], send = b0 ? a[k] : a[k + 4]; a[k] = keep + dpp<XOR1>(send); }
#pragma unroll
      for (int k = 0; k < 2; ++k) { const float keep = b1_ ? a[k + 2] : a[k], send = b1_ ? a[k] : a[k + 2]; a[k] = keep + dpp<XOR2>(send); }
      { const float keep = b3 ? a[1] : a[0], send = b3 ? a[0] : a[1]; a[0] = keep + dpp<XOR8>(send); }
      const float qs = x32_sum(x16_sum(a[0]));
      const int qc = half * 8 + (lane & 1) * 4 + ((lane >> 1) & 1) * 2 + ((lane >> 3) & 1);
      LAS float* QP = (LAS float*)(L + L_QP);
      if (lane < 16) QP[cw * 16 + qc] = qs;
      WAIT_LGKM0();
      int old = 0;
      if (lane == 0) old = __hip_atomic_fetch_add((LAS int*)&C[C_QCNT], 1, __ATOMIC_RELAXED, __HIP_MEMORY_SCOPE_WORKGROUP);
      if (rfl(old) == NC - 1) {
        asm volatile("" ::: "memory");
        if (lane < 8) {
          const f32x2 p0 = *(LAS f32x2*)(QP + 2 * lane), p1 = *(LAS f32x2*)(QP + 16 + 2 * lane), p2 = *(LAS f32x2*)(QP + 32 + 2 * lane), p3 = *(LAS f32x2*)(QP + 48 + 2 * lane);
          const unsigned pq = pk_bf16((p0[0] + p1[0]) + (p2[0] + p3[0]), (p0[1] + p1[1]) + (p2[1] + p3[1]));
          __hip_atomic_store((GAS unsigned long long*)(ws + W_QG) + (col0 >> 1) + lane, ((unsigned long long)tag << 32) | pq, __ATOMIC_RELAXED, __HIP_MEMORY_SCOPE_AGENT);
        }
        STAMP(2);
      }
    }
    const int hp = b >> 3, jc = b & 7, r3 = lane >> 3, p = lane & 7;
    v4u wov[4];
#pragma unroll
    for (int k = 0; k < 4; ++k) wov[k] = *(const GAS v4u*)(wo + (size_t)(hp * 128 + 32 * cw + 8 * k + r3) * 512 + 64 * jc + 8 * p);

    const int g4 = lane >> 4, r16 = lane & 15;
    unsigned kaddr[4];
#pragma unroll
    for (int s = 0; s < 4; ++s) kaddr[s] = r16 * 256 + (((4 * s + g4) ^ r16) * 16);
    const int vrow = 4 * g4 + ((lane >> 2) & 3);
    const unsigned vaddr0 = 8192 + vrow * 256 + (vrow & 7) * 32 + (lane & 3) * 8;
    v4u qf[4];
    f32x4 O[8];
#pragma unroll
    for (int d = 0; d < 8; ++d) O[d] = f32x4{0.f, 0.f, 0.f, 0.f};
    float mrun = -INFINITY, lsum = 0.f;
    constexpr float SCALE = 0.08838834764831845f, LOG2E = 1.4426950408889634f;
    bool dead = false;
    int slot = cw;
    for (int t = cw; t < TPW; t += NC) {
      bool ok; SPIN(rfl(C[C_FULL]) > t, 0, ok);
      if (!ok) { dead = true; break; }
      const unsigned sb = slot * SLOT;
      v4u kf[2][4];
      v4u vf[8];
#pragma unroll
      for (int s = 0; s < 4; ++s) {
        kf[0][s] = *(const LAS v4u*)(L + sb + kaddr[s]);
        kf[1][s] = *(const LAS v4u*)(L + sb + kaddr[s] + 4096);
      }
#pragma unroll
      for (int d = 0; d < 8; ++d) {
        const unsigned a = (sb + vaddr0) ^ (d * 32);
        const s16x4 lo = __builtin_amdgcn_ds_read_tr16_b64_v4i16((LAS s16x4*)(L + a));
        const s16x4 hi = __builtin_amdgcn_ds_read_tr16_b64_v4i16((LAS s16x4*)(L + a + 4096));
        const v2u l2 = __builtin_bit_cast(v2u, lo), h2 = __builtin_bit_cast(v2u, hi);
        vf[d] = v4u{l2[0], l2[1], h2[0], h2[1]};
      }
      WAIT_LGKM0();
      C[C_FREE + slot] = t + 1;
      asm volatile("" : "+v"(kf[0][0]), "+v"(kf[0][1]), "+v"(kf[0][2]), "+v"(kf[0][3]), "+v"(kf[1][0]), "+v"(kf[1][1]), "+v"(kf[1][2]), "+v"(kf[1][3]) :: "memory");
      asm volatile("" : "+v"(vf[0]), "+v"(vf[1]), "+v"(vf[2]), "+v"(vf[3]), "+v"(vf[4]), "+v"(vf[5]), "+v"(vf[6]), "+v"(vf[7]) :: "memory");
      slot += NC; if (slot >= NSLOT) slot -= NSLOT;
      if (t == cw) {
        SPIN(rfl(C[C_QRDY]) != 0, 1, ok);
        if (!ok) { dead = true; break; }
#pragma unroll
        for (int s = 0; s < 4; ++s) qf[s] = *(const LAS v4u*)(L + L_Q + (lane & 3) * 256 + s * 64 + g4 * 16);
      }
      f32x4 s0 = {0.f, 0.f, 0.f, 0.f}, s1 = {0.f, 0.f, 0.f, 0.f};
#pragma unroll
      for (int s = 0; s < 4; ++s) { s0 = mfma16(kf[0][s], qf[s], s0); s1 = mfma16(kf[1][s], qf[s], s1); }
      float sc[8];
#pragma unroll
      for (int k = 0; k < 4; k += 2) {
        const unsigned u0 = pk_bf16(s0[k], s0[k + 1]), u1 = pk_bf16(s1[k], s1[k + 1]);
        const unsigned v0 = pk_bf16(bf_lo(u0) * SCALE, bf_hi(u0) * SCALE), v1 = pk_bf16(bf_lo(u1) * SCALE, bf_hi(u1) * SCALE);
        sc[k] = bf_lo(v0) * LOG2E; sc[k + 1] = bf_hi(v0) * LOG2E; sc[4 + k] = bf_lo(v1) * LOG2E; sc[4 + k + 1] = bf_hi(v1) * LOG2E;
      }
      float pm = fmaxf(fmaxf(fmaxf(sc[0], sc[1]), fmaxf(sc[2], sc[3])), fmaxf(fmaxf(sc[4], sc[5]), fmaxf(sc[6], sc[7])));
      if (__any(pm > mrun)) {
        pm = x32_max(x16_max(pm));
        const float mn = fmaxf(mrun, pm);
        const float alpha = __builtin_amdgcn_exp2f(mrun - mn);
        mrun = mn;
        lsum *= alpha;
#pragma unroll
        for (int d = 0; d < 8; ++d) O[d] *= alpha;
      }
      float pr[8];
#pragma unroll
      for (int k = 0; k < 8; ++k) pr[k] = __builtin_amdgcn_exp2f(sc[k] - mrun);
      lsum += ((pr[0] + pr[1]) + (pr[2] + pr[3])) + ((pr[4] + pr[5]) + (pr[6] + pr[7]));
      const v4u pf = {pk_bf16(pr[0], pr[1]), pk_bf16(pr[2], pr[3]), pk_bf16(pr[4], pr[5]), pk_bf16(pr[6], pr[7])};
#pragma unroll
      for (int d = 0; d < 8; ++d) O[d] = mfma16(vf[d], pf, O[d]);
    }
    if (dead) { C[C_ABORT] = 1; return; }
    if (cw == 0) STAMP(3);
    lsum = x32_sum(x16_sum(lsum));
    LAS float* ML = (LAS float*)(L + L_ML);
    LAS float* OB = (LAS float*)(L + L_O);
    if (r16 < 4) {
#pragma unroll
      for (int d = 0; d < 8; ++d) *(LAS f32x4*)(OB + (cw * 4 + r16) * 128 + 16 * d + 4 * g4) = O[d];
      if (g4 == 0) *(LAS f32x2*)(ML + (cw * 4 + r16) * 2) = f32x2{mrun, lsum};
    }
    WAIT_LGKM0();
    if (lane == 0) __hip_atomic_fetch_add((LAS int*)&C[C_MCNT], 1, __ATOMIC_RELAXED, __HIP_MEMORY_SCOPE_WORKGROUP);
    {
      bool ok; SPIN(rfl(C[C_MCNT]) >= NC, 0, ok);
      if (!ok) { C[C_ABORT] = 1; return; }
    }
    {
      const int d0 = (lane >> 4) * 32 + ((lane >> 3) & 1) * 16 + (lane & 7);
      float mw[NC], lw[NC], o0[NC], o1[NC];
#pragma unroll
      for (int w = 0; w < NC; ++w) {
        const f32x2 ml = *(LAS f32x2*)(ML + (w * 4 + cw) * 2);
        mw[w] = ml[0]; lw[w] = ml[1];
        o0[w] = OB[(w * 4 + cw) * 128 + d0]; o1[w] = OB[(w * 4 + cw) * 128 + d0 + 8];
      }
      const float M = fmaxf(fmaxf(mw[0], mw[1]), fmaxf(mw[2], mw[3]));
      float Ls = 0.f, a0 = 0.f, a1 = 0.f;
#pragma unroll
      for (int w = 0; w < NC; ++w) { const float wt = __builtin_amdgcn_exp2f(mw[w] - M); Ls = fmaf(wt, lw[w], Ls); a0 = fmaf(wt, o0[w], a0); a1 = fmaf(wt, o1[w], a1); }
      const float inv = 1.f / Ls;
      const int h = 4 * G + cw;
      const v4u gr = {__float_as_uint(a0 * inv), tag, __float_as_uint(a1 * inv), tag};
      __builtin_amdgcn_raw_buffer_store_b128(gr, rs, W_P1 + ((h * 4 + (lane >> 4)) * 32 + gi) * 256 + (lane & 15) * 16, 0, AUX_SC1);
      if (lane == 0) {
        const v2u lg = {__float_as_uint(M + __builtin_amdgcn_logf(Ls)), tag};
        __builtin_amdgcn_raw_buffer_store_b64(lg, rs, W_LSE + (h * 32 + (gi & 7) * 4 + (gi >> 3)) * 8, 0, AUX_SC1);
      }
    }
    if (cw == 0) STAMP(4);
    unsigned wp[2][8];
#pragma unroll
    for (int kk = 0; kk < 2; ++kk)
#pragma unroll
      for (int k = 0; k < 4; ++k) {
        wp[kk][2 * k] = __builtin_amdgcn_perm(wov[2 * kk + 1][k], wov[2 * kk][k], 0x05040100u);
        wp[kk][2 * k + 1] = __builtin_amdgcn_perm(wov[2 * kk + 1][k], wov[2 * kk][k], 0x07060302u);
      }
    v4u pd[4][2], pl[2];
    {
      const unsigned dbase = W_P1 + ((hp * 4 + cw) * 32 + p) * 256 + r3 * 16;
      const unsigned lbase = W_LSE + (hp * 32 + p * 4) * 8;
      auto pass = [&]() -> bool {
#pragma unroll
        for (int i = 0; i < 4; ++i) {
          pd[i][0] = __builtin_amdgcn_raw_buffer_load_b128(rs, dbase + i * 2048, 0, AUX_SC1);
          pd[i][1] = __builtin_amdgcn_raw_buffer_load_b128(rs, dbase + i * 2048 + 128, 0, AUX_SC1);
        }
        pl[0] = __builtin_amdgcn_raw_buffer_load_b128(rs, lbase, 0, AUX_SC1);
        pl[1] = __builtin_amdgcn_raw_buffer_load_b128(rs, lbase + 16, 0, AUX_SC1);
        unsigned bad = (pl[0][1] ^ tag) | (pl[0][3] ^ tag) | (pl[1][1] ^ tag) | (pl[1][3] ^ tag);
#pragma unroll
        for (int i = 0; i < 4; ++i) bad |= (pd[i][0][1] ^ tag) | (pd[i][0][3] ^ tag) | (pd[i][1][1] ^ tag) | (pd[i][1][3] ^ tag);
        return __all(bad == 0u);
      };
      bool ok; SPIN(pass(), 4, ok);
      if (!ok) { C[C_ABORT] = 1; return; }
    }
    if (cw == 0) STAMP(5);
    {
      const float e0 = __uint_as_float(pl[0][0]), e1 = __uint_as_float(pl[0][2]), e2 = __uint_as_float(pl[1][0]), e3 = __uint_as_float(pl[1][2]);
      float M = fmaxf(fmaxf(e0, e1), fmaxf(e2, e3));
      M = fmaxf(M, dpp<XOR1>(M)); M = fmaxf(M, dpp<XOR2>(M)); M = fmaxf(M, dpp<XOR7>(M));
      const float wt[4] = {__builtin_amdgcn_exp2f(e0 - M), __builtin_amdgcn_exp2f(e1 - M), __builtin_amdgcn_exp2f(e2 - M), __builtin_amdgcn_exp2f(e3 - M)};
      float Ls = (wt[0] + wt[1]) + (wt[2] + wt[3]);
      float o[4] = {0.f, 0.f, 0.f, 0.f};
#pragma unroll
      for (int i = 0; i < 4; ++i) {
        o[0] = fmaf(wt[i], __uint_as_float(pd[i][0][0]), o[0]); o[1] = fmaf(wt[i], __uint_as_float(pd[i][0][2]), o[1]);
        o[2] = fmaf(wt[i], __uint_as_float(pd[i][1][0]), o[2]); o[3] = fmaf(wt[i], __uint_as_float(pd[i][1][2]), o[3]);
      }
      Ls += dpp<XOR1>(Ls); Ls += dpp<XOR2>(Ls); Ls += dpp<XOR7>(Ls);
#pragma unroll
      for (int k = 0; k < 4; ++k) { o[k] += dpp<XOR1>(o[k]); o[k] += dpp<XOR2>(o[k]); o[k] += dpp<XOR7>(o[k]); }
      const float inv = 1.f / Ls;
      const unsigned op0 = pk_bf16(o[0] * inv, o[1] * inv), op1 = pk_bf16(o[2] * inv, o[3] * inv);
      float y[8];
#pragma unroll
      for (int n = 0; n < 8; ++n) y[n] = dot2(wp[1][n], op1, dot2(wp[0][n], op0, 0.f));
      const bool b3 = lane & 8, b4 = lane & 16, b5 = lane & 32;
#pragma unroll
      for (int k = 0; k < 4; ++k) { const float keep = b3 ? y[k + 4] : y[k], send = b3 ? y[k] : y[k + 4]; y[k] = keep + dpp<XOR8>(send); }
#pragma unroll
      for (int k = 0; k < 2; ++k) {
        auto s = __builtin_amdgcn_permlane16_swap(__float_as_uint(y[k]), __float_as_uint(y[k + 2]), false, false);
        y[k] = __uint_as_float(s[0]) + __uint_as_float(s[1]);
      }
      {
        auto s = __builtin_amdgcn_permlane32_swap(__float_as_uint(y[0]), __float_as_uint(y[1]), false, false);
        y[0] = __uint_as_float(s[0]) + __uint_as_float(s[1]);
      }
      (void)b4; (void)b5;
      const int n = 8 * p + ((lane >> 3) & 1) * 4 + ((lane >> 4) & 1) * 2 + (lane >> 5);
      LAS float* Y = (LAS float*)(L + L_Y);
      Y[cw * 64 + n] = y[0];
      WAIT_LGKM0();
      int old = 0;
      if (lane == 0) old = __hip_atomic_fetch_add((LAS int*)&C[C_YCNT], 1, __ATOMIC_RELAXED, __HIP_MEMORY_SCOPE_WORKGROUP);
      if (rfl(old) == NC - 1) {
        asm volatile("" ::: "memory");
        if (lane < 32) {
          const f32x2 p0 = *(LAS f32x2*)(Y + 2 * lane), p1 = *(LAS f32x2*)(Y + 64 + 2 * lane), p2 = *(LAS f32x2*)(Y + 128 + 2 * lane), p3 = *(LAS f32x2*)(Y + 192 + 2 * lane);
          const v4u gr = {__float_as_uint((p0[0] + p1[0]) + (p2[0] + p3[0])), tag, __float_as_uint((p0[1] + p1[1]) + (p2[1] + p3[1])), tag};
          __builtin_amdgcn_raw_buffer_store_b128(gr, rs, W_P2 + ((jc * 32 + lane) * 32 + hp) * 16, 0, AUX_SC1);
        }
        STAMP(6);
      }
    }
    return;
  }

  const unsigned tag = __hip_atomic_load((GAS unsigned*)(ws + W_EPOCH), __ATOMIC_RELAXED, __HIP_MEMORY_SCOPE_AGENT) + 1u;
  const v4u xr = *(const GAS v4u*)(x + 8 * lane);
  __builtin_amdgcn_s_barrier();
  bool ok;
  {
    v4u g0, g1;
    auto pass = [&]() -> bool {
      g0 = __builtin_amdgcn_raw_buffer_load_b128(rs, W_QG + G * 2048 + lane * 32, 0, AUX_SC1);
      g1 = __builtin_amdgcn_raw_buffer_load_b128(rs, W_QG + G * 2048 + lane * 32 + 16, 0, AUX_SC1);
      return __all(((g0[1] ^ tag) | (g0[3] ^ tag) | (g1[1] ^ tag) | (g1[3] ^ tag)) == 0u);
    };
    SPIN(pass(), 2, ok);
    if (!ok) { C[C_ABORT] = 1; return; }
    *(LAS v4u*)(L + L_Q + lane * 16) = v4u{g0[0], g0[2], g1[0], g1[2]};
    WAIT_LGKM0();
    C[C_QRDY] = 1;
    STAMP(7);
  }
  float gate0, gate1, x0, x1;
  {
    float xv[8];
#pragma unroll
    for (int k = 0; k < 4; ++k) { xv[2 * k] = bf_lo(xr[k]); xv[2 * k + 1] = bf_hi(xr[k]); }
    float m, den, m2, den2;
    ln_stats(xv, m, den);
#pragma unroll
    for (int k = 0; k < 8; ++k) xv[k] = ln_apply(xv[k], m, den);
    ln_stats(xv, m2, den2);
    const unsigned xd = *(const GAS unsigned*)(x + 2 * b);
    x0 = bf_lo(xd); x1 = bf_hi(xd);
    const float h0 = ln_apply(ln_apply(x0, m, den), m2, den2), h1 = ln_apply(ln_apply(x1, m, den), m2, den2);
    float a0 = 0.f, a1 = 0.f;
    auto gelu = [](float u) {
      const float z2 = -2.f * 0.7978845608028654f * 1.4426950408889634f * fmaf(0.044715f * u * u, u, u);
      return rbf(u * __builtin_amdgcn_rcpf(1.f + __builtin_amdgcn_exp2f(z2)));
    };
#pragma unroll
    for (int i = 0; i < 7; ++i) {
      const int f = 4 * (lane + 64 * i);
      const v2u a = *(const GAS v2u*)(w1 + f), bb = *(const GAS v2u*)(b1 + f), cc = *(const GAS v2u*)(w2 + f);
#pragma unroll
      for (int k = 0; k < 2; ++k) {
        a0 = fmaf(gelu(rbf(rbf(h0 * bf_lo(a[k])) + bf_lo(bb[k]))), bf_lo(cc[k]), a0);
        a0 = fmaf(gelu(rbf(rbf(h0 * bf_hi(a[k])) + bf_hi(bb[k]))), bf_hi(cc[k]), a0);
        a1 = fmaf(gelu(rbf(rbf(h1 * bf_lo(a[k])) + bf_lo(bb[k]))), bf_lo(cc[k]), a1);
        a1 = fmaf(gelu(rbf(rbf(h1 * bf_hi(a[k])) + bf_hi(bb[k]))), bf_hi(cc[k]), a1);
      }
    }
    const float bias2 = bf_lo((unsigned)b2[0]);
    const float y0 = rbf(rbf(wave_sum(a0)) + bias2), y1 = rbf(rbf(wave_sum(a1)) + bias2);
    gate0 = rbf(y0 / (1.f + __expf(-y0)));
    gate1 = rbf(y1 / (1.f + __expf(-y1)));
  }
  STAMP(8);
  SPIN(rfl(C[C_MCNT]) >= NC, 8, ok);
  if (!ok) return;
  {
    v4u g;
    auto pass = [&]() -> bool {
      g = __builtin_amdgcn_raw_buffer_load_b128(rs, W_P2 + (b * 32 + (lane & 31)) * 16, 0, AUX_SC1);
      return __all(((g[1] ^ tag) | (g[3] ^ tag)) == 0u);
    };
    SPIN(pass(), 2, ok);
    if (!ok) return;
    float s0 = __uint_as_float(g[0]), s1 = __uint_as_float(g[2]);
    s0 += dpp<XOR1>(s0); s0 += dpp<XOR2>(s0); s0 += dpp<XOR7>(s0); s0 += dpp<XOR8>(s0); s0 = x16_sum(s0);
    s1 += dpp<XOR1>(s1); s1 += dpp<XOR2>(s1); s1 += dpp<XOR7>(s1); s1 += dpp<XOR8>(s1); s1 = x16_sum(s1);
    if (lane == 0) {
      *(GAS unsigned*)(out + 2 * b) = pk_bf16(x0 + rbf(rbf(s0) * gate0), x1 + rbf(rbf(s1) * gate1));
      if (b == 0) __hip_atomic_store((GAS unsigned*)(ws + W_EPOCH), tag, __ATOMIC_RELAXED, __HIP_MEMORY_SCOPE_AGENT);
    }
    STAMP(9);
  }
}
}

extern "C" void kernel_launch(void* const* d_in, const int* in_sizes, int n_in, void* d_out, int out_size, void* d_ws, size_t ws_size,
                              hipStream_t stream) {
  for (int i = 0; i < n_in; ++i) {
    hipPointerAttribute_t at; hipError_t e = hipPointerGetAttributes(&at, d_in[i]);
    void* base = nullptr; size_t sz = 0; hipError_t e2 = hipMemGetAddressRange((hipDeviceptr_t*)&base, &sz, d_in[i]);
    fprintf(stderr, "in[%d] %p n=%d attr e=%d type=%d managed=%d flags=%u | range e=%d base %p size %zu\n", i, d_in[i], in_sizes[i], (int)e, (int)at.type, (int)at.isManaged, at.allocationFlags, (int)e2, base, sz);
  }
  {
    void* base = nullptr; size_t sz = 0; hipError_t e2 = hipMemGetAddressRange((hipDeviceptr_t*)&base, &sz, d_ws);
    fprintf(stderr, "ws %p size %zu range e=%d base %p size %zu; out %p\n", d_ws, ws_size, (int)e2, base, sz, d_out);
  }
  (void)hipFuncSetAttribute((const void*)fused_decode, hipFuncAttributeMaxDynamicSharedMemorySize, LDS_BYTES);
  hipLaunchKernelGGL(fused_decode, dim3(NWG), dim3(NTHR), LDS_BYTES, stream, (const u16*)d_in[0], (const u16*)d_in[1], (const u16*)d_in[2],
                     (const u16*)d_in[3], (const u16*)d_in[4], (const u16*)d_in[6], (const u16*)d_in[7], (const u16*)d_in[8],
                     (const u16*)d_in[9], (u16*)d_out, (unsigned char*)d_ws);
}
```

```cpp
#define DIAG 12
#define DIAGB 0
#include <hip/hip_runtime.h>
#include <cstdint>

#define GAS __attribute__((address_space(1)))
#define LAS __attribute__((address_space(3)))
typedef unsigned short u16;
typedef unsigned v4u __attribute__((ext_vector_type(4)));
typedef unsigned v2u __attribute__((ext_vector_type(2)));
typedef float f32x4 __attribute__((ext_vector_type(4)));
typedef float f32x2 __attribute__((ext_vector_type(2)));
typedef short s16x4 __attribute__((ext_vector_type(4)));
typedef __bf16 bf16x8 __attribute__((ext_vector_type(8)));
typedef __bf16 bf16x2 __attribute__((ext_vector_type(2)));

namespace {
constexpr int D = 512, HQ = 32, HKV = 8, SK = 65536, DH = 128, FF = 1792;
constexpr int NWG = 256, NC = 4, NWAVE = NC + 2, NTHR = NWAVE * 64;
constexpr int TK = 32;
constexpr int SLOT = 2 * TK * 256;
constexpr int NSLOT = 9;
constexpr int TPW = SK / 32 / TK;
constexpr int L_RING = 0;
constexpr int L_CTRL = NSLOT * SLOT;
constexpr int C_FULL = 0, C_TOTAL = 1, C_QRDY = 2, C_QCNT = 3, C_MCNT = 4, C_YCNT = 5, C_ABORT = 6, C_FREE = 16;
constexpr int L_Q = L_CTRL + 256;
constexpr int L_QP = L_Q + 1024;
constexpr int L_ML = L_QP + 256;
constexpr int L_Y = L_ML + 128;
constexpr int L_O = L_Y + 1024;
constexpr int LDS_BYTES = L_O + 8192;
static_assert(LDS_BYTES <= 160 * 1024, "LDS");
constexpr unsigned W_EPOCH = 0;
constexpr unsigned W_QG = 4096;
constexpr unsigned W_LSE = W_QG + 8 * 256 * 8;
constexpr unsigned W_P2 = W_LSE + 32 * 32 * 8;
constexpr unsigned W_P1 = W_P2 + 256 * 64 * 8;
constexpr unsigned W_END = W_P1 + 32 * 4 * 32 * 32 * 8;
constexpr unsigned W_STAMP = W_END;
constexpr int AUX_SC1 = 16, AUX_NT = 2;
constexpr unsigned SPIN_CAP = 1u << 22;

__device__ __forceinline__ float bf_lo(unsigned u) { return __uint_as_float(u << 16); }
__device__ __forceinline__ float bf_hi(unsigned u) { return __uint_as_float(u & 0xffff0000u); }
__device__ __forceinline__ unsigned pk_bf16(float lo, float hi) {
  bf16x2 r = {(__bf16)lo, (__bf16)hi};
  return __builtin_bit_cast(unsigned, r);
}
__device__ __forceinline__ float rbf(float f) { return bf_lo(pk_bf16(f, f)); }
template <int CTRL> __device__ __forceinline__ float dpp(float x) {
  return __builtin_bit_cast(float, __builtin_amdgcn_mov_dpp(__builtin_bit_cast(int, x), CTRL, 0xf, 0xf, true));
}
constexpr int XOR1 = 0xB1, XOR2 = 0x4E, XOR7 = 0x141, XOR8 = 0x128;
__device__ __forceinline__ float x16_sum(float x) { auto s = __builtin_amdgcn_permlane16_swap(__float_as_uint(x), __float_as_uint(x), false, false); return __uint_as_float(s[0]) + __uint_as_float(s[1]); }
__device__ __forceinline__ float x32_sum(float x) { auto s = __builtin_amdgcn_permlane32_swap(__float_as_uint(x), __float_as_uint(x), false, false); return __uint_as_float(s[0]) + __uint_as_float(s[1]); }
__device__ __forceinline__ float x16_max(float x) { auto s = __builtin_amdgcn_permlane16_swap(__float_as_uint(x), __float_as_uint(x), false, false); return fmaxf(__uint_as_float(s[0]), __uint_as_float(s[1])); }
__device__ __forceinline__ float x32_max(float x) { auto s = __builtin_amdgcn_permlane32_swap(__float_as_uint(x), __float_as_uint(x), false, false); return fmaxf(__uint_as_float(s[0]), __uint_as_float(s[1])); }
__device__ __forceinline__ float wave_sum(float v) {
  v += dpp<XOR1>(v); v += dpp<XOR2>(v); v += dpp<XOR7>(v); v += dpp<XOR8>(v);
  return x32_sum(x16_sum(v));
}
__device__ __forceinline__ float dot2(unsigned a, unsigned b, float acc) {
  return __builtin_amdgcn_fdot2_f32_bf16(__builtin_bit_cast(bf16x2, a), __builtin_bit_cast(bf16x2, b), acc, false);
}
__device__ __forceinline__ f32x4 mfma16(v4u a, v4u b, f32x4 c) {
  return __builtin_amdgcn_mfma_f32_16x16x32_bf16(__builtin_bit_cast(bf16x8, a), __builtin_bit_cast(bf16x8, b), c, 0, 0, 0);
}
#define VPOL 16
#define GLDS(src, dst, off) __builtin_amdgcn_global_load_lds((const GAS unsigned*)(src), (LAS unsigned*)(dst), 16, (off), AUX_NT)
#define GLDV(src, dst, off) __builtin_amdgcn_global_load_lds((const GAS unsigned*)(src), (LAS unsigned*)(dst), 16, (off), VPOL)
#define WAIT_VM(n) asm volatile("s_waitcnt vmcnt(" #n ")" ::: "memory")
#define WAIT_LGKM0() asm volatile("s_waitcnt lgkmcnt(0)" ::: "memory")
__device__ __forceinline__ int rfl(int v) { return __builtin_amdgcn_readfirstlane(v); }

__device__ __forceinline__ void ln_stats(const float (&v)[8], float& m, float& den) {
  float s = 0.f;
#pragma unroll
  for (int k = 0; k < 8; ++k) s += v[k];
  const float mean = wave_sum(s) * (1.f / D);
  float q = 0.f;
#pragma unroll
  for (int k = 0; k < 8; ++k) { const float d = v[k] - mean; q = fmaf(d, d, q); }
  const float var = wave_sum(q) * (1.f / D);
  m = rbf(mean);
  den = rbf(sqrtf(rbf(rbf(var) + 1e-5f)));
}
__device__ __forceinline__ float ln_apply(float x, float m, float den) { return rbf(rbf(x - m) / den); }

#define SPIN(cond, sleepn, okvar) do { unsigned sp_ = 0; okvar = true; _Pragma("nounroll") for (;;) { asm volatile("" ::: "memory"); if (cond) break; \
    if (sleepn) __builtin_amdgcn_s_sleep(sleepn); \
    if ((++sp_ & 63u) == 0u && (sp_ > SPIN_CAP || rfl(C[C_ABORT]) != 0)) { okvar = false; break; } } asm volatile("" ::: "memory"); } while (0)

#define STAMP(i) do { C[32 + (i)] = (int)(unsigned)__builtin_amdgcn_s_memrealtime(); } while (0)

__global__ void __launch_bounds__(NTHR, 2) fused_decode(const u16* __restrict__ x, const u16* __restrict__ kc, const u16* __restrict__ vc,
                                                         const u16* __restrict__ wq, const u16* __restrict__ wo, const u16* __restrict__ w1,
                                                         const u16* __restrict__ b1, const u16* __restrict__ w2, const u16* __restrict__ b2,
                                                         u16* __restrict__ out, unsigned char* __restrict__ ws) {
  extern __shared__ __attribute__((aligned(16))) unsigned char smem[];
  LAS unsigned char* L = (LAS unsigned char*)smem;
  volatile LAS int* C = (volatile LAS int*)(L + L_CTRL);
  const int tid = threadIdx.x, lane = tid & 63, wave = rfl(tid >> 6), b = blockIdx.x;
  const int G = b >> 5, gi = b & 31;
  const __amdgpu_buffer_rsrc_t rs = __builtin_amdgcn_make_buffer_rsrc((void*)ws, 0, (int)W_END, 0x00020000);

  if (__builtin_expect(wave == 0, 1)) {
    const unsigned tstart = (unsigned)__builtin_amdgcn_s_memrealtime();
    const GAS unsigned char* kb = (const GAS unsigned char*)kc + ((size_t)G * SK + (size_t)gi * (TPW * TK)) * 256;
    const GAS unsigned char* vb = (const GAS unsigned char*)vc + ((size_t)G * SK + (size_t)gi * (TPW * TK)) * 256;
    const int r4 = lane >> 4, c = lane & 15;
    unsigned ko[4], vo[2];
#pragma unroll
    for (int j = 0; j < 4; ++j) ko[j] = r4 * 256 + ((c ^ (4 * j + r4)) * 16);
#pragma unroll
    for (int j = 0; j < 2; ++j) vo[j] = r4 * 256 + ((c ^ ((4 * j + r4) << 1)) * 16);
    auto issue = [&](int t, int s) {
      const GAS unsigned char* ks = kb + (size_t)t * (TK * 256);
      const GAS unsigned char* vs = vb + (size_t)t * (TK * 256);
      LAS unsigned char* dst = L + s * SLOT;
      GLDS(ks + ko[0], dst, 0); GLDS(ks + ko[1], dst, 1024); GLDS(ks + ko[2], dst, 2048); GLDS(ks + ko[3], dst, 3072);
      GLDS(ks + 4096 + ko[0], dst + 4096, 0); GLDS(ks + 4096 + ko[1], dst + 4096, 1024); GLDS(ks + 4096 + ko[2], dst + 4096, 2048); GLDS(ks + 4096 + ko[3], dst + 4096, 3072);
      GLDV(vs + vo[0], dst + 8192, 0); GLDV(vs + vo[1], dst + 8192, 1024); GLDV(vs + vo[0], dst + 8192, 2048); GLDV(vs + vo[1], dst + 8192, 3072);
      GLDV(vs + 4096 + vo[0], dst + 12288, 0); GLDV(vs + 4096 + vo[1], dst + 12288, 1024); GLDV(vs + 4096 + vo[0], dst + 12288, 2048); GLDV(vs + 4096 + vo[1], dst + 12288, 3072);
    };
    __builtin_amdgcn_s_barrier();
    C[32] = (int)tstart;
    issue(0, 0);
    issue(1, 1);
    issue(2, 2);
    WAIT_VM(32); C[C_FULL] = 1;
    STAMP(12);
    int pub = 1, s = 3;
    bool dead = false;
    for (int t = 3; t < TPW; ++t) {
      if (t >= NSLOT) {
        const int need = t - NSLOT + 1;
        if (rfl(C[C_FREE + s]) < need) {
          if (pub < t - 1) { WAIT_VM(16); pub = t - 1; C[C_FULL] = pub; }
          WAIT_VM(0); pub = t; C[C_FULL] = pub;
          bool ok; SPIN(rfl(C[C_FREE + s]) >= need, 1, ok);
          if (!ok) { dead = true; break; }
        }
        asm volatile("" ::: "memory");
      }
      issue(t, s);
      if (pub < t - 1) { WAIT_VM(32); pub = t - 1; C[C_FULL] = pub; }
      s = (s + 1 == NSLOT) ? 0 : s + 1;
    }
    if (dead) { WAIT_VM(0); C[C_ABORT] = 1; return; }
    WAIT_VM(16); C[C_FULL] = TPW - 1;
    WAIT_VM(0); C[C_FULL] = TPW;
    STAMP(1);
    return;
  }

  if (wave <= NC) {
    const int cw = wave - 1;
    const int jrow = (lane & 3) | ((lane >> 3) << 2), half = (lane >> 2) & 1;
    const int col0 = G * 512 + ((gi & 7) * 4 + (gi >> 3)) * 16;
    v4u wv[4];
#pragma unroll
    for (int i = 0; i < 4; ++i) wv[i] = *(const GAS v4u*)(wq + (size_t)(16 * jrow + 4 * cw + i) * 4096 + col0 + 8 * half);
    const v2u xq = *(const GAS v2u*)(x + 16 * jrow + 4 * cw);
    const v4u xr = *(const GAS v4u*)(x + 8 * lane);
    const unsigned tag = __hip_atomic_load((GAS unsigned*)(ws + W_EPOCH), __ATOMIC_RELAXED, __HIP_MEMORY_SCOPE_AGENT) + 1u;
    if (tid < 64 + 64) { C[lane] = (lane == C_TOTAL) ? TPW : 0; }
    WAIT_LGKM0();
    __builtin_amdgcn_sched_barrier(0);
    __builtin_amdgcn_s_barrier();
    __builtin_amdgcn_sched_barrier(0);
    if (cw == 0) STAMP(10);

    {
      float xv[8];
#pragma unroll
      for (int k = 0; k < 4; ++k) { xv[2 * k] = bf_lo(xr[k]); xv[2 * k + 1] = bf_hi(xr[k]); }
      float m, den;
      ln_stats(xv, m, den);
      float xn[4] = {ln_apply(bf_lo(xq[0]), m, den), ln_apply(bf_hi(xq[0]), m, den), ln_apply(bf_lo(xq[1]), m, den), ln_apply(bf_hi(xq[1]), m, den)};
      float a[8];
#pragma unroll
      for (int k = 0; k < 8; ++k) a[k] = 0.f;
#pragma unroll
      for (int i = 0; i < 4; ++i)
#pragma unroll
        for (int k = 0; k < 4; ++k) { a[2 * k] = fmaf(xn[i], bf_lo(wv[i][k]), a[2 * k]); a[2 * k + 1] = fmaf(xn[i], bf_hi(wv[i][k]), a[2 * k + 1]); }
      const bool b0 = lane & 1, b1_ = lane & 2, b3 = lane & 8;
#pragma unroll
      for (int k = 0; k < 4; ++k) { const float keep = b0 ? a[k + 4] : a[k], send = b0 ? a[k] : a[k + 4]; a[k] = keep + dpp<XOR1>(send); }
#pragma unroll
      for (int k = 0; k < 2; ++k) { const float keep = b1_ ? a[k + 2] : a[k], send = b1_ ? a[k] : a[k + 2]; a[k] = keep + dpp<XOR2>(send); }
      { const float keep = b3 ? a[1] : a[0], send = b3 ? a[0] : a[1]; a[0] = keep + dpp<XOR8>(send); }
      const float qs = x32_sum(x16_sum(a[0]));
      const int qc = half * 8 + (lane & 1) * 4 + ((lane >> 1) & 1) * 2 + ((lane >> 3) & 1);
      LAS float* QP = (LAS float*)(L + L_QP);
      if (lane < 16) QP[cw * 16 + qc] = qs;
      WAIT_LGKM0();
      int old = 0;
      if (lane == 0) old = __hip_atomic_fetch_add((LAS int*)&C[C_QCNT], 1, __ATOMIC_RELAXED, __HIP_MEMORY_SCOPE_WORKGROUP);
      if (rfl(old) == NC - 1) {
        asm volatile("" ::: "memory");
        if (lane < 8) {
          const f32x2 p0 = *(LAS f32x2*)(QP + 2 * lane), p1 = *(LAS f32x2*)(QP + 16 + 2 * lane), p2 = *(LAS f32x2*)(QP + 32 + 2 * lane), p3 = *(LAS f32x2*)(QP + 48 + 2 * lane);
          const unsigned pq = pk_bf16((p0[0] + p1[0]) + (p2[0] + p3[0]), (p0[1] + p1[1]) + (p2[1] + p3[1]));
          __hip_atomic_store((GAS unsigned long long*)(ws + W_QG) + (col0 >> 1) + lane, ((unsigned long long)tag << 32) | pq, __ATOMIC_RELAXED, __HIP_MEMORY_SCOPE_AGENT);
        }
        STAMP(2);
      }
    }
    const int hp = b >> 3, jc = b & 7, r3 = lane >> 3, p = lane & 7;
    v4u wov[4];
#pragma unroll
    for (int k = 0; k < 4; ++k) wov[k] = *(const GAS v4u*)(wo + (size_t)(hp * 128 + 32 * cw + 8 * k + r3) * 512 + 64 * jc + 8 * p);

    const int g4 = lane >> 4, r16 = lane & 15;
    unsigned kaddr[4];
#pragma unroll
    for (int s = 0; s < 4; ++s) kaddr[s] = r16 * 256 + (((4 * s + g4) ^ r16) * 16);
    const int vrow = 4 * g4 + ((lane >> 2) & 3);
    const unsigned vaddr0 = 8192 + vrow * 256 + (vrow & 7) * 32 + (lane & 3) * 8;
    v4u qf[4];
    f32x4 O[8];
#pragma unroll
    for (int d = 0; d < 8; ++d) O[d] = f32x4{0.f, 0.f, 0.f, 0.f};
    float mrun = -INFINITY, lsum = 0.f;
    constexpr float SCALE = 0.08838834764831845f, LOG2E = 1.4426950408889634f;
    bool dead = false;
    int slot = cw;
    for (int t = cw; t < TPW; t += NC) {
      bool ok; SPIN(rfl(C[C_FULL]) > t, 0, ok);
      if (!ok) { dead = true; break; }
      const unsigned sb = slot * SLOT;
      v4u kf[2][4];
      v4u vf[8];
#pragma unroll
      for (int s = 0; s < 4; ++s) {
        kf[0][s] = *(const LAS v4u*)(L + sb + kaddr[s]);
        kf[1][s] = *(const LAS v4u*)(L + sb + kaddr[s] + 4096);
      }
#pragma unroll
      for (int d = 0; d < 8; ++d) {
        const unsigned a = (sb + vaddr0) ^ (d * 32);
        const s16x4 lo = __builtin_amdgcn_ds_read_tr16_b64_v4i16((LAS s16x4*)(L + a));
        const s16x4 hi = __builtin_amdgcn_ds_read_tr16_b64_v4i16((LAS s16x4*)(L + a + 4096));
        const v2u l2 = __builtin_bit_cast(v2u, lo), h2 = __builtin_bit_cast(v2u, hi);
        vf[d] = v4u{l2[0], l2[1], h2[0], h2[1]};
      }
      WAIT_LGKM0();
      C[C_FREE + slot] = t + 1;
      asm volatile("" : "+v"(kf[0][0]), "+v"(kf[0][1]), "+v"(kf[0][2]), "+v"(kf[0][3]), "+v"(kf[1][0]), "+v"(kf[1][1]), "+v"(kf[1][2]), "+v"(kf[1][3]) :: "memory");
      asm volatile("" : "+v"(vf[0]), "+v"(vf[1]), "+v"(vf[2]), "+v"(vf[3]), "+v"(vf[4]), "+v"(vf[5]), "+v"(vf[6]), "+v"(vf[7]) :: "memory");
      slot += NC; if (slot >= NSLOT) slot -= NSLOT;
      if (t == cw) {
        SPIN(rfl(C[C_QRDY]) != 0, 1, ok);
        if (!ok) { dead = true; break; }
#pragma unroll
        for (int s = 0; s < 4; ++s) qf[s] = *(const LAS v4u*)(L + L_Q + (lane & 3) * 256 + s * 64 + g4 * 16);
      }
      f32x4 s0 = {0.f, 0.f, 0.f, 0.f}, s1 = {0.f, 0.f, 0.f, 0.f};
#pragma unroll
      for (int s = 0; s < 4; ++s) { s0 = mfma16(kf[0][s], qf[s], s0); s1 = mfma16(kf[1][s], qf[s], s1); }
      float sc[8];
#pragma unroll
      for (int k = 0; k < 4; k += 2) {
        const unsigned u0 = pk_bf16(s0[k], s0[k + 1]), u1 = pk_bf16(s1[k], s1[k + 1]);
        const unsigned v0 = pk_bf16(bf_lo(u0) * SCALE, bf_hi(u0) * SCALE), v1 = pk_bf16(bf_lo(u1) * SCALE, bf_hi(u1) * SCALE);
        sc[k] = bf_lo(v0) * LOG2E; sc[k + 1] = bf_hi(v0) * LOG2E; sc[4 + k] = bf_lo(v1) * LOG2E; sc[4 + k + 1] = bf_hi(v1) * LOG2E;
      }
      float pm = fmaxf(fmaxf(fmaxf(sc[0], sc[1]), fmaxf(sc[2], sc[3])), fmaxf(fmaxf(sc[4], sc[5]), fmaxf(sc[6], sc[7])));
      if (__any(pm > mrun)) {
        pm = x32_max(x16_max(pm));
        const float mn = fmaxf(mrun, pm);
        const float alpha = __builtin_amdgcn_exp2f(mrun - mn);
        mrun = mn;
        lsum *= alpha;
#pragma unroll
        for (int d = 0; d < 8; ++d) O[d] *= alpha;
      }
      float pr[8];
#pragma unroll
      for (int k = 0; k < 8; ++k) pr[k] = __builtin_amdgcn_exp2f(sc[k] - mrun);
      lsum += ((pr[0] + pr[1]) + (pr[2] + pr[3])) + ((pr[4] + pr[5]) + (pr[6] + pr[7]));
      const v4u pf = {pk_bf16(pr[0], pr[1]), pk_bf16(pr[2], pr[3]), pk_bf16(pr[4], pr[5]), pk_bf16(pr[6], pr[7])};
#pragma unroll
      for (int d = 0; d < 8; ++d) O[d] = mfma16(vf[d], pf, O[d]);
    }
    if (dead) { C[C_ABORT] = 1; return; }
    if (cw == 0) STAMP(3);
    lsum = x32_sum(x16_sum(lsum));
    LAS float* ML = (LAS float*)(L + L_ML);
    LAS float* OB = (LAS float*)(L + L_O);
    if (r16 < 4) {
#pragma unroll
      for (int d = 0; d < 8; ++d) *(LAS f32x4*)(OB + (cw * 4 + r16) * 128 + 16 * d + 4 * g4) = O[d];
      if (g4 == 0) *(LAS f32x2*)(ML + (cw * 4 + r16) * 2) = f32x2{mrun, lsum};
    }
    WAIT_LGKM0();
    if (lane == 0) __hip_atomic_fetch_add((LAS int*)&C[C_MCNT], 1, __ATOMIC_RELAXED, __HIP_MEMORY_SCOPE_WORKGROUP);
    {
      bool ok; SPIN(rfl(C[C_MCNT]) >= NC, 0, ok);
      if (!ok) { C[C_ABORT] = 1; return; }
    }
    {
      const int d0 = (lane >> 4) * 32 + ((lane >> 3) & 1) * 16 + (lane & 7);
      float mw[NC], lw[NC], o0[NC], o1[NC];
#pragma unroll
      for (int w = 0; w < NC; ++w) {
        const f32x2 ml = *(LAS f32x2*)(ML + (w * 4 + cw) * 2);
        mw[w] = ml[0]; lw[w] = ml[1];
        o0[w] = OB[(w * 4 + cw) * 128 + d0]; o1[w] = OB[(w * 4 + cw) * 128 + d0 + 8];
      }
      const float M = fmaxf(fmaxf(mw[0], mw[1]), fmaxf(mw[2], mw[3]));
      float Ls = 0.f, a0 = 0.f, a1 = 0.f;
#pragma unroll
      for (int w = 0; w < NC; ++w) { const float wt = __builtin_amdgcn_exp2f(mw[w] - M); Ls = fmaf(wt, lw[w], Ls); a0 = fmaf(wt, o0[w], a0); a1 = fmaf(wt, o1[w], a1); }
      const float inv = 1.f / Ls;
      const int h = 4 * G + cw;
      const v4u gr = {__float_as_uint(a0 * inv), tag, __float_as_uint(a1 * inv), tag};
      __builtin_amdgcn_raw_buffer_store_b128(gr, rs, W_P1 + ((h * 4 + (lane >> 4)) * 32 + gi) * 256 + (lane & 15) * 16, 0, AUX_SC1);
      if (lane == 0) {
        const v2u lg = {__float_as_uint(M + __builtin_amdgcn_logf(Ls)), tag};
        __builtin_amdgcn_raw_buffer_store_b64(lg, rs, W_LSE + (h * 32 + (gi & 7) * 4 + (gi >> 3)) * 8, 0, AUX_SC1);
      }
    }
    if (cw == 0) STAMP(4);
    unsigned wp[2][8];
#pragma unroll
    for (int kk = 0; kk < 2; ++kk)
#pragma unroll
      for (int k = 0; k < 4; ++k) {
        wp[kk][2 * k] = __builtin_amdgcn_perm(wov[2 * kk + 1][k], wov[2 * kk][k], 0x05040100u);
        wp[kk][2 * k + 1] = __builtin_amdgcn_perm(wov[2 * kk + 1][k], wov[2 * kk][k], 0x07060302u);
      }
    v4u pd[4][2], pl[2];
    {
      const unsigned dbase = W_P1 + ((hp * 4 + cw) * 32 + p) * 256 + r3 * 16;
      const unsigned lbase = W_LSE + (hp * 32 + p * 4) * 8;
      auto pass = [&]() -> bool {
#pragma unroll
        for (int i = 0; i < 4; ++i) {
          pd[i][0] = __builtin_amdgcn_raw_buffer_load_b128(rs, dbase + i * 2048, 0, AUX_SC1);
          pd[i][1] = __builtin_amdgcn_raw_buffer_load_b128(rs, dbase + i * 2048 + 128, 0, AUX_SC1);
        }
        pl[0] = __builtin_amdgcn_raw_buffer_load_b128(rs, lbase, 0, AUX_SC1);
        pl[1] = __builtin_amdgcn_raw_buffer_load_b128(rs, lbase + 16, 0, AUX_SC1);
        unsigned bad = (pl[0][1] ^ tag) | (pl[0][3] ^ tag) | (pl[1][1] ^ tag) | (pl[1][3] ^ tag);
#pragma unroll
        for (int i = 0; i < 4; ++i) bad |= (pd[i][0][1] ^ tag) | (pd[i][0][3] ^ tag) | (pd[i][1][1] ^ tag) | (pd[i][1][3] ^ tag);
        return __all(bad == 0u);
      };
      bool ok; SPIN(pass(), 4, ok);
      if (!ok) { C[C_ABORT] = 1; return; }
    }
    if (cw == 0) STAMP(5);
    {
      const float e0 = __uint_as_float(pl[0][0]), e1 = __uint_as_float(pl[0][2]), e2 = __uint_as_float(pl[1][0]), e3 = __uint_as_float(pl[1][2]);
      float M = fmaxf(fmaxf(e0, e1), fmaxf(e2, e3));
      M = fmaxf(M, dpp<XOR1>(M)); M = fmaxf(M, dpp<XOR2>(M)); M = fmaxf(M, dpp<XOR7>(M));
      const float wt[4] = {__builtin_amdgcn_exp2f(e0 - M), __builtin_amdgcn_exp2f(e1 - M), __builtin_amdgcn_exp2f(e2 - M), __builtin_amdgcn_exp2f(e3 - M)};
      float Ls = (wt[0] + wt[1]) + (wt[2] + wt[3]);
      float o[4] = {0.f, 0.f, 0.f, 0.f};
#pragma unroll
      for (int i = 0; i < 4; ++i) {
        o[0] = fmaf(wt[i], __uint_as_float(pd[i][0][0]), o[0]); o[1] = fmaf(wt[i], __uint_as_float(pd[i][0][2]), o[1]);
        o[2] = fmaf(wt[i], __uint_as_float(pd[i][1][0]), o[2]); o[3] = fmaf(wt[i], __uint_as_float(pd[i][1][2]), o[3]);
      }
      Ls += dpp<XOR1>(Ls); Ls += dpp<XOR2>(Ls); Ls += dpp<XOR7>(Ls);
#pragma unroll
      for (int k = 0; k < 4; ++k) { o[k] += dpp<XOR1>(o[k]); o[k] += dpp<XOR2>(o[k]); o[k] += dpp<XOR7>(o[k]); }
      const float inv = 1.f / Ls;
      const unsigned op0 = pk_bf16(o[0] * inv, o[1] * inv), op1 = pk_bf16(o[2] * inv, o[3] * inv);
      float y[8];
#pragma unroll
      for (int n = 0; n < 8; ++n) y[n] = dot2(wp[1][n], op1, dot2(wp[0][n], op0, 0.f));
      const bool b3 = lane & 8, b4 = lane & 16, b5 = lane & 32;
#pragma unroll
      for (int k = 0; k < 4; ++k) { const float keep = b3 ? y[k + 4] : y[k], send = b3 ? y[k] : y[k + 4]; y[k] = keep + dpp<XOR8>(send); }
#pragma unroll
      for (int k = 0; k < 2; ++k) {
        auto s = __builtin_amdgcn_permlane16_swap(__float_as_uint(y[k]), __float_as_uint(y[k + 2]), false, false);
        y[k] = __uint_as_float(s[0]) + __uint_as_float(s[1]);
      }
      {
        auto s = __builtin_amdgcn_permlane32_swap(__float_as_uint(y[0]), __float_as_uint(y[1]), false, false);
        y[0] = __uint_as_float(s[0]) + __uint_as_float(s[1]);
      }
      (void)b4; (void)b5;
      const int n = 8 * p + ((lane >> 3) & 1) * 4 + ((lane >> 4) & 1) * 2 + (lane >> 5);
      LAS float* Y = (LAS float*)(L + L_Y);
      Y[cw * 64 + n] = y[0];
      WAIT_LGKM0();
      int old = 0;
      if (lane == 0) old = __hip_atomic_fetch_add((LAS int*)&C[C_YCNT], 1, __ATOMIC_RELAXED, __HIP_MEMORY_SCOPE_WORKGROUP);
      if (rfl(old) == NC - 1) {
        asm volatile("" ::: "memory");
        if (lane < 32) {
          const f32x2 p0 = *(LAS f32x2*)(Y + 2 * lane), p1 = *(LAS f32x2*)(Y + 64 + 2 * lane), p2 = *(LAS f32x2*)(Y + 128 + 2 * lane), p3 = *(LAS f32x2*)(Y + 192 + 2 * lane);
          const v4u gr = {__float_as_uint((p0[0] + p1[0]) + (p2[0] + p3[0])), tag, __float_as_uint((p0[1] + p1[1]) + (p2[1] + p3[1])), tag};
          __builtin_amdgcn_raw_buffer_store_b128(gr, rs, W_P2 + ((jc * 32 + lane) * 32 + hp) * 16, 0, AUX_SC1);
        }
        STAMP(6);
      }
    }
    return;
  }

  const unsigned tag = __hip_atomic_load((GAS unsigned*)(ws + W_EPOCH), __ATOMIC_RELAXED, __HIP_MEMORY_SCOPE_AGENT) + 1u;
  const v4u xr = *(const GAS v4u*)(x + 8 * lane);
  __builtin_amdgcn_s_barrier();
  bool ok;
  {
    v4u g0, g1;
    auto pass = [&]() -> bool {
      g0 = __builtin_amdgcn_raw_buffer_load_b128(rs, W_QG + G * 2048 + lane * 32, 0, AUX_SC1);
      g1 = __builtin_amdgcn_raw_buffer_load_b128(rs, W_QG + G * 2048 + lane * 32 + 16, 0, AUX_SC1);
      return __all(((g0[1] ^ tag) | (g0[3] ^ tag) | (g1[1] ^ tag) | (g1[3] ^ tag)) == 0u);
    };
    SPIN(pass(), 2, ok);
    if (!ok) { C[C_ABORT] = 1; return; }
    *(LAS v4u*)(L + L_Q + lane * 16) = v4u{g0[0], g0[2], g1[0], g1[2]};
    WAIT_LGKM0();
    C[C_QRDY] = 1;
    STAMP(7);
  }
  float gate0, gate1, x0, x1;
  {
    float xv[8];
#pragma unroll
    for (int k = 0; k < 4; ++k) { xv[2 * k] = bf_lo(xr[k]); xv[2 * k + 1] = bf_hi(xr[k]); }
    float m, den, m2, den2;
    ln_stats(xv, m, den);
#pragma unroll
    for (int k = 0; k < 8; ++k) xv[k] = ln_apply(xv[k], m, den);
    ln_stats(xv, m2, den2);
    const unsigned xd = *(const GAS unsigned*)(x + 2 * b);
    x0 = bf_lo(xd); x1 = bf_hi(xd);
    const float h0 = ln_apply(ln_apply(x0, m, den), m2, den2), h1 = ln_apply(ln_apply(x1, m, den), m2, den2);
    float a0 = 0.f, a1 = 0.f;
    auto gelu = [](float u) {
      const float z2 = -2.f * 0.7978845608028654f * 1.4426950408889634f * fmaf(0.044715f * u * u, u, u);
      return rbf(u * __builtin_amdgcn_rcpf(1.f + __builtin_amdgcn_exp2f(z2)));
    };
#pragma unroll
    for (int i = 0; i < 7; ++i) {
      const int f = 4 * (lane + 64 * i);
      const v2u a = *(const GAS v2u*)(w1 + f), bb = *(const GAS v2u*)(b1 + f), cc = *(const GAS v2u*)(w2 + f);
#pragma unroll
      for (int k = 0; k < 2; ++k) {
        a0 = fmaf(gelu(rbf(rbf(h0 * bf_lo(a[k])) + bf_lo(bb[k]))), bf_lo(cc[k]), a0);
        a0 = fmaf(gelu(rbf(rbf(h0 * bf_hi(a[k])) + bf_hi(bb[k]))), bf_hi(cc[k]), a0);
        a1 = fmaf(gelu(rbf(rbf(h1 * bf_lo(a[k])) + bf_lo(bb[k]))), bf_lo(cc[k]), a1);
        a1 = fmaf(gelu(rbf(rbf(h1 * bf_hi(a[k])) + bf_hi(bb[k]))), bf_hi(cc[k]), a1);
      }
    }
    const float bias2 = bf_lo((unsigned)b2[0]);
    const float y0 = rbf(rbf(wave_sum(a0)) + bias2), y1 = rbf(rbf(wave_sum(a1)) + bias2);
    gate0 = rbf(y0 / (1.f + __expf(-y0)));
    gate1 = rbf(y1 / (1.f + __expf(-y1)));
  }
  STAMP(8);
  SPIN(rfl(C[C_MCNT]) >= NC, 8, ok);
  if (!ok) return;
  {
    v4u g;
    auto pass = [&]() -> bool {
      g = __builtin_amdgcn_raw_buffer_load_b128(rs, W_P2 + (b * 32 + (lane & 31)) * 16, 0, AUX_SC1);
      return __all(((g[1] ^ tag) | (g[3] ^ tag)) == 0u);
    };
    SPIN(pass(), 2, ok);
    if (!ok) return;
    float s0 = __uint_as_float(g[0]), s1 = __uint_as_float(g[2]);
    s0 += dpp<XOR1>(s0); s0 += dpp<XOR2>(s0); s0 += dpp<XOR7>(s0); s0 += dpp<XOR8>(s0); s0 = x16_sum(s0);
    s1 += dpp<XOR1>(s1); s1 += dpp<XOR2>(s1); s1 += dpp<XOR7>(s1); s1 += dpp<XOR8>(s1); s1 = x16_sum(s1);
    if (lane == 0) {
      *(GAS unsigned*)(out + 2 * b) = pk_bf16(x0 + rbf(rbf(s0) * gate0), x1 + rbf(rbf(s1) * gate1));
      if (b == 0) __hip_atomic_store((GAS unsigned*)(ws + W_EPOCH), tag, __ATOMIC_RELAXED, __HIP_MEMORY_SCOPE_AGENT);
    }
    STAMP(9);
    if (b == DIAGB) {
      WAIT_LGKM0();
      const unsigned dt = (unsigned)rfl(C[32 + DIAG]) - (unsigned)rfl(C[32]);
      const unsigned t1 = (unsigned)__builtin_amdgcn_s_memrealtime();
      while ((unsigned)__builtin_amdgcn_s_memrealtime() - t1 < 10u * dt) __builtin_amdgcn_s_sleep(8);
    }
  }
}
}

extern "C" void kernel_launch(void* const* d_in, const int* in_sizes, int n_in, void* d_out, int out_size, void* d_ws, size_t ws_size,
                              hipStream_t stream) {
  (void)hipFuncSetAttribute((const void*)fused_decode, hipFuncAttributeMaxDynamicSharedMemorySize, LDS_BYTES);
  hipLaunchKernelGGL(fused_decode, dim3(NWG), dim3(NTHR), LDS_BYTES, stream, (const u16*)d_in[0], (const u16*)d_in[1], (const u16*)d_in[2],
                     (const u16*)d_in[3], (const u16*)d_in[4], (const u16*)d_in[6], (const u16*)d_in[7], (const u16*)d_in[8],
                     (const u16*)d_in[9], (u16*)d_out, (unsigned char*)d_ws);
}
```

```cpp
#define DIAG 1
#define PADALL 0
#define TOPSTAMP 0
#define PRETINY 0
#define POSTTINY 0
#define VPOL 2
#include <hip/hip_runtime.h>
#include <cstdint>

#define GAS __attribute__((address_space(1)))
#define LAS __attribute__((address_space(3)))
typedef unsigned short u16;
typedef unsigned v4u __attribute__((ext_vector_type(4)));
typedef unsigned v2u __attribute__((ext_vector_type(2)));
typedef float f32x4 __attribute__((ext_vector_type(4)));
typedef float f32x2 __attribute__((ext_vector_type(2)));
typedef short s16x4 __attribute__((ext_vector_type(4)));
typedef __bf16 bf16x8 __attribute__((ext_vector_type(8)));
typedef __bf16 bf16x2 __attribute__((ext_vector_type(2)));

namespace {
constexpr int D = 512, HQ = 32, HKV = 8, SK = 65536, DH = 128, FF = 1792;
constexpr int NWG = 256, NC = 4, NWAVE = NC + 2, NTHR = NWAVE * 64;
constexpr int TK = 32;
constexpr int SLOT = 2 * TK * 256;
constexpr int NSLOT = 9;
constexpr int TPW = SK / 32 / TK;
constexpr int L_RING = 0;
constexpr int L_CTRL = NSLOT * SLOT;
constexpr int C_FULL = 0, C_TOTAL = 1, C_QRDY = 2, C_QCNT = 3, C_MCNT = 4, C_YCNT = 5, C_ABORT = 6, C_FREE = 16;
constexpr int L_Q = L_CTRL + 256;
constexpr int L_QP = L_Q + 1024;
constexpr int L_ML = L_QP + 256;
constexpr int L_Y = L_ML + 128;
constexpr int L_O = L_Y + 1024;
constexpr int LDS_BYTES = L_O + 8192;
static_assert(LDS_BYTES <= 160 * 1024, "LDS");
constexpr unsigned W_EPOCH = 0;
constexpr unsigned W_QG = 4096;
constexpr unsigned W_LSE = W_QG + 8 * 256 * 8;
constexpr unsigned W_P2 = W_LSE + 32 * 32 * 8;
constexpr unsigned W_P1 = W_P2 + 256 * 64 * 8;
constexpr unsigned W_END = W_P1 + 32 * 4 * 32 * 32 * 8;
constexpr unsigned W_STAMP = W_END;
constexpr int AUX_SC1 = 16, AUX_NT = 2;
constexpr unsigned SPIN_CAP = 1u << 22;

__device__ __forceinline__ float bf_lo(unsigned u) { return __uint_as_float(u << 16); }
__device__ __forceinline__ float bf_hi(unsigned u) { return __uint_as_float(u & 0xffff0000u); }
__device__ __forceinline__ unsigned pk_bf16(float lo, float hi) {
  bf16x2 r = {(__bf16)lo, (__bf16)hi};
  return __builtin_bit_cast(unsigned, r);
}
__device__ __forceinline__ float rbf(float f) { return bf_lo(pk_bf16(f, f)); }
template <int CTRL> __device__ __forceinline__ float dpp(float x) {
  return __builtin_bit_cast(float, __builtin_amdgcn_mov_dpp(__builtin_bit_cast(int, x), CTRL, 0xf, 0xf, true));
}
constexpr int XOR1 = 0xB1, XOR2 = 0x4E, XOR7 = 0x141, XOR8 = 0x128;
__device__ __forceinline__ float x16_sum(float x) { auto s = __builtin_amdgcn_permlane16_swap(__float_as_uint(x), __float_as_uint(x), false, false); return __uint_as_float(s[0]) + __uint_as_float(s[1]); }
__device__ __forceinline__ float x32_sum(float x) { auto s = __builtin_amdgcn_permlane32_swap(__float_as_uint(x), __float_as_uint(x), false, false); return __uint_as_float(s[0]) + __uint_as_float(s[1]); }
__device__ __forceinline__ float x16_max(float x) { auto s = __builtin_amdgcn_permlane16_swap(__float_as_uint(x), __float_as_uint(x), false, false); return fmaxf(__uint_as_float(s[0]), __uint_as_float(s[1])); }
__device__ __forceinline__ float x32_max(float x) { auto s = __builtin_amdgcn_permlane32_swap(__float_as_uint(x), __float_as_uint(x), false, false); return fmaxf(__uint_as_float(s[0]), __uint_as_float(s[1])); }
__device__ __forceinline__ float wave_sum(float v) {
  v += dpp<XOR1>(v); v += dpp<XOR2>(v); v += dpp<XOR7>(v); v += dpp<XOR8>(v);
  return x32_sum(x16_sum(v));
}
__device__ __forceinline__ float dot2(unsigned a, unsigned b, float acc) {
  return __builtin_amdgcn_fdot2_f32_bf16(__builtin_bit_cast(bf16x2, a), __builtin_bit_cast(bf16x2, b), acc, false);
}
__device__ __forceinline__ f32x4 mfma16(v4u a, v4u b, f32x4 c) {
  return __builtin_amdgcn_mfma_f32_16x16x32_bf16(__builtin_bit_cast(bf16x8, a), __builtin_bit_cast(bf16x8, b), c, 0, 0, 0);
}
#define GLDS(src, dst, off) __builtin_amdgcn_global_load_lds((const GAS unsigned*)(src), (LAS unsigned*)(dst), 16, (off), AUX_NT)
#define GLDV(src, dst, off) __builtin_amdgcn_global_load_lds((const GAS unsigned*)(src), (LAS unsigned*)(dst), 16, (off), VPOL)
#define WAIT_VM(n) asm volatile("s_waitcnt vmcnt(" #n ")" ::: "memory")
#define WAIT_LGKM0() asm volatile("s_waitcnt lgkmcnt(0)" ::: "memory")
__device__ __forceinline__ int rfl(int v) { return __builtin_amdgcn_readfirstlane(v); }

__device__ __forceinline__ void ln_stats(const float (&v)[8], float& m, float& den) {
  float s = 0.f;
#pragma unroll
  for (int k = 0; k < 8; ++k) s += v[k];
  const float mean = wave_sum(s) * (1.f / D);
  float q = 0.f;
#pragma unroll
  for (int k = 0; k < 8; ++k) { const float d = v[k] - mean; q = fmaf(d, d, q); }
  const float var = wave_sum(q) * (1.f / D);
  m = rbf(mean);
  den = rbf(sqrtf(rbf(rbf(var) + 1e-5f)));
}
__device__ __forceinline__ float ln_apply(float x, float m, float den) { return rbf(rbf(x - m) / den); }

#define SPIN(cond, sleepn, okvar) do { unsigned sp_ = 0; okvar = true; _Pragma("nounroll") for (;;) { asm volatile("" ::: "memory"); if (cond) break; \
    if (sleepn) __builtin_amdgcn_s_sleep(sleepn); \
    if ((++sp_ & 63u) == 0u && (sp_ > SPIN_CAP || rfl(C[C_ABORT]) != 0)) { okvar = false; break; } } asm volatile("" ::: "memory"); } while (0)

#define STAMP(i) do { C[32 + (i)] = (int)(unsigned)__builtin_amdgcn_s_memrealtime(); } while (0)

__global__ void __launch_bounds__(NTHR, 2) fused_decode(const u16* __restrict__ x, const u16* __restrict__ kc, const u16* __restrict__ vc,
                                                         const u16* __restrict__ wq, const u16* __restrict__ wo, const u16* __restrict__ w1,
                                                         const u16* __restrict__ b1, const u16* __restrict__ w2, const u16* __restrict__ b2,
                                                         u16* __restrict__ out, unsigned char* __restrict__ ws) {
  const unsigned ttop = (unsigned)__builtin_amdgcn_s_memrealtime();
  __builtin_amdgcn_sched_barrier(0);
  extern __shared__ __attribute__((aligned(16))) unsigned char smem[];
  LAS unsigned char* L = (LAS unsigned char*)smem;
  volatile LAS int* C = (volatile LAS int*)(L + L_CTRL);
  const int tid = threadIdx.x, lane = tid & 63, wave = rfl(tid >> 6), b = blockIdx.x;
  const int G = b >> 5, gi = b & 31;
  const __amdgpu_buffer_rsrc_t rs = __builtin_amdgcn_make_buffer_rsrc((void*)ws, 0, (int)W_END, 0x00020000);

  if (__builtin_expect(wave == 0, 1)) {
    const unsigned tstart = (unsigned)__builtin_amdgcn_s_memrealtime();
    const GAS unsigned char* kb = (const GAS unsigned char*)kc + ((size_t)G * SK + (size_t)gi * (TPW * TK)) * 256;
    const GAS unsigned char* vb = (const GAS unsigned char*)vc + ((size_t)G * SK + (size_t)gi * (TPW * TK)) * 256;
    const int r4 = lane >> 4, c = lane & 15;
    unsigned ko[4], vo[2];
#pragma unroll
    for (int j = 0; j < 4; ++j) ko[j] = r4 * 256 + ((c ^ (4 * j + r4)) * 16);
#pragma unroll
    for (int j = 0; j < 2; ++j) vo[j] = r4 * 256 + ((c ^ ((4 * j + r4) << 1)) * 16);
    auto issue = [&](int t, int s) {
      const GAS unsigned char* ks = kb + (size_t)t * (TK * 256);
      const GAS unsigned char* vs = vb + (size_t)t * (TK * 256);
      LAS unsigned char* dst = L + s * SLOT;
      GLDS(ks + ko[0], dst, 0); GLDS(ks + ko[1], dst, 1024); GLDS(ks + ko[2], dst, 2048); GLDS(ks + ko[3], dst, 3072);
      GLDS(ks + 4096 + ko[0], dst + 4096, 0); GLDS(ks + 4096 + ko[1], dst + 4096, 1024); GLDS(ks + 4096 + ko[2], dst + 4096, 2048); GLDS(ks + 4096 + ko[3], dst + 4096, 3072);
      GLDV(vs + vo[0], dst + 8192, 0); GLDV(vs + vo[1], dst + 8192, 1024); GLDV(vs + vo[0], dst + 8192, 2048); GLDV(vs + vo[1], dst + 8192, 3072);
      GLDV(vs + 4096 + vo[0], dst + 12288, 0); GLDV(vs + 4096 + vo[1], dst + 12288, 1024); GLDV(vs + 4096 + vo[0], dst + 12288, 2048); GLDV(vs + 4096 + vo[1], dst + 12288, 3072);
    };
    __builtin_amdgcn_s_barrier();
    C[32] = (int)tstart;
    issue(0, 0);
    issue(1, 1);
    issue(2, 2);
    WAIT_VM(32); C[C_FULL] = 1;
    STAMP(12);
    int pub = 1, s = 3;
    bool dead = false;
    for (int t = 3; t < TPW; ++t) {
      if (t >= NSLOT) {
        const int need = t - NSLOT + 1;
        if (rfl(C[C_FREE + s]) < need) {
          if (pub < t - 1) { WAIT_VM(16); pub = t - 1; C[C_FULL] = pub; }
          WAIT_VM(0); pub = t; C[C_FULL] = pub;
          bool ok; SPIN(rfl(C[C_FREE + s]) >= need, 1, ok);
          if (!ok) { dead = true; break; }
        }
        asm volatile("" ::: "memory");
      }
      issue(t, s);
      if (pub < t - 1) { WAIT_VM(32); pub = t - 1; C[C_FULL] = pub; }
      s = (s + 1 == NSLOT) ? 0 : s + 1;
    }
    if (dead) { WAIT_VM(0); C[C_ABORT] = 1; return; }
    WAIT_VM(16); C[C_FULL] = TPW - 1;
    WAIT_VM(0); C[C_FULL] = TPW;
    STAMP(1);
    return;
  }

  if (wave <= NC) {
    const int cw = wave - 1;
    const int jrow = (lane & 3) | ((lane >> 3) << 2), half = (lane >> 2) & 1;
    const int col0 = G * 512 + ((gi & 7) * 4 + (gi >> 3)) * 16;
    v4u wv[4];
#pragma unroll
    for (int i = 0; i < 4; ++i) wv[i] = *(const GAS v4u*)(wq + (size_t)(16 * jrow + 4 * cw + i) * 4096 + col0 + 8 * half);
    const v2u xq = *(const GAS v2u*)(x + 16 * jrow + 4 * cw);
    const v4u xr = *(const GAS v4u*)(x + 8 * lane);
    const unsigned tag = __hip_atomic_load((GAS unsigned*)(ws + W_EPOCH), __ATOMIC_RELAXED, __HIP_MEMORY_SCOPE_AGENT) + 1u;
    if (tid < 64 + 64) { C[lane] = (lane == C_TOTAL) ? TPW : 0; }
    WAIT_LGKM0();
    __builtin_amdgcn_sched_barrier(0);
    __builtin_amdgcn_s_barrier();
    __builtin_amdgcn_sched_barrier(0);
    if (cw == 0) STAMP(10);

    {
      float xv[8];
#pragma unroll
      for (int k = 0; k < 4; ++k) { xv[2 * k] = bf_lo(xr[k]); xv[2 * k + 1] = bf_hi(xr[k]); }
      float m, den;
      ln_stats(xv, m, den);
      float xn[4] = {ln_apply(bf_lo(xq[0]), m, den), ln_apply(bf_hi(xq[0]), m, den), ln_apply(bf_lo(xq[1]), m, den), ln_apply(bf_hi(xq[1]), m, den)};
      float a[8];
#pragma unroll
      for (int k = 0; k < 8; ++k) a[k] = 0.f;
#pragma unroll
      for (int i = 0; i < 4; ++i)
#pragma unroll
        for (int k = 0; k < 4; ++k) { a[2 * k] = fmaf(xn[i], bf_lo(wv[i][k]), a[2 * k]); a[2 * k + 1] = fmaf(xn[i], bf_hi(wv[i][k]), a[2 * k + 1]); }
      const bool b0 = lane & 1, b1_ = lane & 2, b3 = lane & 8;
#pragma unroll
      for (int k = 0; k < 4; ++k) { const float keep = b0 ? a[k + 4] : a[k], send = b0 ? a[k] : a[k + 4]; a[k] = keep + dpp<XOR1>(send); }
#pragma unroll
      for (int k = 0; k < 2; ++k) { const float keep = b1_ ? a[k + 2] : a[k], send = b1_ ? a[k] : a[k + 2]; a[k] = keep + dpp<XOR2>(send); }
      { const float keep = b3 ? a[1] : a[0], send = b3 ? a[0] : a[1]; a[0] = keep + dpp<XOR8>(send); }
      const float qs = x32_sum(x16_sum(a[0]));
      const int qc = half * 8 + (lane & 1) * 4 + ((lane >> 1) & 1) * 2 + ((lane >> 3) & 1);
      LAS float* QP = (LAS float*)(L + L_QP);
      if (lane < 16) QP[cw * 16 + qc] = qs;
      WAIT_LGKM0();
      int old = 0;
      if (lane == 0) old = __hip_atomic_fetch_add((LAS int*)&C[C_QCNT], 1, __ATOMIC_RELAXED, __HIP_MEMORY_SCOPE_WORKGROUP);
      if (rfl(old) == NC - 1) {
        asm volatile("" ::: "memory");
        if (lane < 8) {
          const f32x2 p0 = *(LAS f32x2*)(QP + 2 * lane), p1 = *(LAS f32x2*)(QP + 16 + 2 * lane), p2 = *(LAS f32x2*)(QP + 32 + 2 * lane), p3 = *(LAS f32x2*)(QP + 48 + 2 * lane);
          const unsigned pq = pk_bf16((p0[0] + p1[0]) + (p2[0] + p3[0]), (p0[1] + p1[1]) + (p2[1] + p3[1]));
          __hip_atomic_store((GAS unsigned long long*)(ws + W_QG) + (col0 >> 1) + lane, ((unsigned long long)tag << 32) | pq, __ATOMIC_RELAXED, __HIP_MEMORY_SCOPE_AGENT);
        }
        STAMP(2);
      }
    }
    const int hp = b >> 3, jc = b & 7, r3 = lane >> 3, p = lane & 7;
    v4u wov[4];
#pragma unroll
    for (int k = 0; k < 4; ++k) wov[k] = *(const GAS v4u*)(wo + (size_t)(hp * 128 + 32 * cw + 8 * k + r3) * 512 + 64 * jc + 8 * p);

    const int g4 = lane >> 4, r16 = lane & 15;
    unsigned kaddr[4];
#pragma unroll
    for (int s = 0; s < 4; ++s) kaddr[s] = r16 * 256 + (((4 * s + g4) ^ r16) * 16);
    const int vrow = 4 * g4 + ((lane >> 2) & 3);
    const unsigned vaddr0 = 8192 + vrow * 256 + (vrow & 7) * 32 + (lane & 3) * 8;
    v4u qf[4];
    f32x4 O[8];
#pragma unroll
    for (int d = 0; d < 8; ++d) O[d] = f32x4{0.f, 0.f, 0.f, 0.f};
    float mrun = -INFINITY, lsum = 0.f;
    constexpr float SCALE = 0.08838834764831845f, LOG2E = 1.4426950408889634f;
    bool dead = false;
    int slot = cw;
    for (int t = cw; t < TPW; t += NC) {
      bool ok; SPIN(rfl(C[C_FULL]) > t, 0, ok);
      if (!ok) { dead = true; break; }
      const unsigned sb = slot * SLOT;
      v4u kf[2][4];
      v4u vf[8];
#pragma unroll
      for (int s = 0; s < 4; ++s) {
        kf[0][s] = *(const LAS v4u*)(L + sb + kaddr[s]);
        kf[1][s] = *(const LAS v4u*)(L + sb + kaddr[s] + 4096);
      }
#pragma unroll
      for (int d = 0; d < 8; ++d) {
        const unsigned a = (sb + vaddr0) ^ (d * 32);
        const s16x4 lo = __builtin_amdgcn_ds_read_tr16_b64_v4i16((LAS s16x4*)(L + a));
        const s16x4 hi = __builtin_amdgcn_ds_read_tr16_b64_v4i16((LAS s16x4*)(L + a + 4096));
        const v2u l2 = __builtin_bit_cast(v2u, lo), h2 = __builtin_bit_cast(v2u, hi);
        vf[d] = v4u{l2[0], l2[1], h2[0], h2[1]};
      }
      WAIT_LGKM0();
      C[C_FREE + slot] = t + 1;
      asm volatile("" : "+v"(kf[0][0]), "+v"(kf[0][1]), "+v"(kf[0][2]), "+v"(kf[0][3]), "+v"(kf[1][0]), "+v"(kf[1][1]), "+v"(kf[1][2]), "+v"(kf[1][3]) :: "memory");
      asm volatile("" : "+v"(vf[0]), "+v"(vf[1]), "+v"(vf[2]), "+v"(vf[3]), "+v"(vf[4]), "+v"(vf[5]), "+v"(vf[6]), "+v"(vf[7]) :: "memory");
      slot += NC; if (slot >= NSLOT) slot -= NSLOT;
      if (t == cw) {
        SPIN(rfl(C[C_QRDY]) != 0, 1, ok);
        if (!ok) { dead = true; break; }
#pragma unroll
        for (int s = 0; s < 4; ++s) qf[s] = *(const LAS v4u*)(L + L_Q + (lane & 3) * 256 + s * 64 + g4 * 16);
      }
      f32x4 s0 = {0.f, 0.f, 0.f, 0.f}, s1 = {0.f, 0.f, 0.f, 0.f};
#pragma unroll
      for (int s = 0; s < 4; ++s) { s0 = mfma16(kf[0][s], qf[s], s0); s1 = mfma16(kf[1][s], qf[s], s1); }
      float sc[8];
#pragma unroll
      for (int k = 0; k < 4; k += 2) {
        const unsigned u0 = pk_bf16(s0[k], s0[k + 1]), u1 = pk_bf16(s1[k], s1[k + 1]);
        const unsigned v0 = pk_bf16(bf_lo(u0) * SCALE, bf_hi(u0) * SCALE), v1 = pk_bf16(bf_lo(u1) * SCALE, bf_hi(u1) * SCALE);
        sc[k] = bf_lo(v0) * LOG2E; sc[k + 1] = bf_hi(v0) * LOG2E; sc[4 + k] = bf_lo(v1) * LOG2E; sc[4 + k + 1] = bf_hi(v1) * LOG2E;
      }
      float pm = fmaxf(fmaxf(fmaxf(sc[0], sc[1]), fmaxf(sc[2], sc[3])), fmaxf(fmaxf(sc[4], sc[5]), fmaxf(sc[6], sc[7])));
      if (__any(pm > mrun)) {
        pm = x32_max(x16_max(pm));
        const float mn = fmaxf(mrun, pm);
        const float alpha = __builtin_amdgcn_exp2f(mrun - mn);
        mrun = mn;
        lsum *= alpha;
#pragma unroll
        for (int d = 0; d < 8; ++d) O[d] *= alpha;
      }
      float pr[8];
#pragma unroll
      for (int k = 0; k < 8; ++k) pr[k] = __builtin_amdgcn_exp2f(sc[k] - mrun);
      lsum += ((pr[0] + pr[1]) + (pr[2] + pr[3])) + ((pr[4] + pr[5]) + (pr[6] + pr[7]));
      const v4u pf = {pk_bf16(pr[0], pr[1]), pk_bf16(pr[2], pr[3]), pk_bf16(pr[4], pr[5]), pk_bf16(pr[6], pr[7])};
#pragma unroll
      for (int d = 0; d < 8; ++d) O[d] = mfma16(vf[d], pf, O[d]);
    }
    if (dead) { C[C_ABORT] = 1; return; }
    if (cw == 0) STAMP(3);
    lsum = x32_sum(x16_sum(lsum));
    LAS float* ML = (LAS float*)(L + L_ML);
    LAS float* OB = (LAS float*)(L + L_O);
    if (r16 < 4) {
#pragma unroll
      for (int d = 0; d < 8; ++d) *(LAS f32x4*)(OB + (cw * 4 + r16) * 128 + 16 * d + 4 * g4) = O[d];
      if (g4 == 0) *(LAS f32x2*)(ML + (cw * 4 + r16) * 2) = f32x2{mrun, lsum};
    }
    WAIT_LGKM0();
    if (lane == 0) __hip_atomic_fetch_add((LAS int*)&C[C_MCNT], 1, __ATOMIC_RELAXED, __HIP_MEMORY_SCOPE_WORKGROUP);
    {
      bool ok; SPIN(rfl(C[C_MCNT]) >= NC, 0, ok);
      if (!ok) { C[C_ABORT] = 1; return; }
    }
    {
      const int d0 = (lane >> 4) * 32 + ((lane >> 3) & 1) * 16 + (lane & 7);
      float mw[NC], lw[NC], o0[NC], o1[NC];
#pragma unroll
      for (int w = 0; w < NC; ++w) {
        const f32x2 ml = *(LAS f32x2*)(ML + (w * 4 + cw) * 2);
        mw[w] = ml[0]; lw[w] = ml[1];
        o0[w] = OB[(w * 4 + cw) * 128 + d0]; o1[w] = OB[(w * 4 + cw) * 128 + d0 + 8];
      }
      const float M = fmaxf(fmaxf(mw[0], mw[1]), fmaxf(mw[2], mw[3]));
      float Ls = 0.f, a0 = 0.f, a1 = 0.f;
#pragma unroll
      for (int w = 0; w < NC; ++w) { const float wt = __builtin_amdgcn_exp2f(mw[w] - M); Ls = fmaf(wt, lw[w], Ls); a0 = fmaf(wt, o0[w], a0); a1 = fmaf(wt, o1[w], a1); }
      const float inv = 1.f / Ls;
      const int h = 4 * G + cw;
      const v4u gr = {__float_as_uint(a0 * inv), tag, __float_as_uint(a1 * inv), tag};
      __builtin_amdgcn_raw_buffer_store_b128(gr, rs, W_P1 + ((h * 4 + (lane >> 4)) * 32 + gi) * 256 + (lane & 15) * 16, 0, AUX_SC1);
      if (lane == 0) {
        const v2u lg = {__float_as_uint(M + __builtin_amdgcn_logf(Ls)), tag};
        __builtin_amdgcn_raw_buffer_store_b64(lg, rs, W_LSE + (h * 32 + (gi & 7) * 4 + (gi >> 3)) * 8, 0, AUX_SC1);
      }
    }
    if (cw == 0) STAMP(4);
    unsigned wp[2][8];
#pragma unroll
    for (int kk = 0; kk < 2; ++kk)
#pragma unroll
      for (int k = 0; k < 4; ++k) {
        wp[kk][2 * k] = __builtin_amdgcn_perm(wov[2 * kk + 1][k], wov[2 * kk][k], 0x05040100u);
        wp[kk][2 * k + 1] = __builtin_amdgcn_perm(wov[2 * kk + 1][k], wov[2 * kk][k], 0x07060302u);
      }
    v4u pd[4][2], pl[2];
    {
      const unsigned dbase = W_P1 + ((hp * 4 + cw) * 32 + p) * 256 + r3 * 16;
      const unsigned lbase = W_LSE + (hp * 32 + p * 4) * 8;
      auto pass = [&]() -> bool {
#pragma unroll
        for (int i = 0; i < 4; ++i) {
          pd[i][0] = __builtin_amdgcn_raw_buffer_load_b128(rs, dbase + i * 2048, 0, AUX_SC1);
          pd[i][1] = __builtin_amdgcn_raw_buffer_load_b128(rs, dbase + i * 2048 + 128, 0, AUX_SC1);
        }
        pl[0] = __builtin_amdgcn_raw_buffer_load_b128(rs, lbase, 0, AUX_SC1);
        pl[1] = __builtin_amdgcn_raw_buffer_load_b128(rs, lbase + 16, 0, AUX_SC1);
        unsigned bad = (pl[0][1] ^ tag) | (pl[0][3] ^ tag) | (pl[1][1] ^ tag) | (pl[1][3] ^ tag);
#pragma unroll
        for (int i = 0; i < 4; ++i) bad |= (pd[i][0][1] ^ tag) | (pd[i][0][3] ^ tag) | (pd[i][1][1] ^ tag) | (pd[i][1][3] ^ tag);
        return __all(bad == 0u);
      };
      bool ok; SPIN(pass(), 4, ok);
      if (!ok) { C[C_ABORT] = 1; return; }
    }
    if (cw == 0) STAMP(5);
    {
      const float e0 = __uint_as_float(pl[0][0]), e1 = __uint_as_float(pl[0][2]), e2 = __uint_as_float(pl[1][0]), e3 = __uint_as_float(pl[1][2]);
      float M = fmaxf(fmaxf(e0, e1), fmaxf(e2, e3));
      M = fmaxf(M, dpp<XOR1>(M)); M = fmaxf(M, dpp<XOR2>(M)); M = fmaxf(M, dpp<XOR7>(M));
      const float wt[4] = {__builtin_amdgcn_exp2f(e0 - M), __builtin_amdgcn_exp2f(e1 - M), __builtin_amdgcn_exp2f(e2 - M), __builtin_amdgcn_exp2f(e3 - M)};
      float Ls = (wt[0] + wt[1]) + (wt[2] + wt[3]);
      float o[4] = {0.f, 0.f, 0.f, 0.f};
#pragma unroll
      for (int i = 0; i < 4; ++i) {
        o[0] = fmaf(wt[i], __uint_as_float(pd[i][0][0]), o[0]); o[1] = fmaf(wt[i], __uint_as_float(pd[i][0][2]), o[1]);
        o[2] = fmaf(wt[i], __uint_as_float(pd[i][1][0]), o[2]); o[3] = fmaf(wt[i], __uint_as_float(pd[i][1][2]), o[3]);
      }
      Ls += dpp<XOR1>(Ls); Ls += dpp<XOR2>(Ls); Ls += dpp<XOR7>(Ls);
#pragma unroll
      for (int k = 0; k < 4; ++k) { o[k] += dpp<XOR1>(o[k]); o[k] += dpp<XOR2>(o[k]); o[k] += dpp<XOR7>(o[k]); }
      const float inv = 1.f / Ls;
      const unsigned op0 = pk_bf16(o[0] * inv, o[1] * inv), op1 = pk_bf16(o[2] * inv, o[3] * inv);
      float y[8];
#pragma unroll
      for (int n = 0; n < 8; ++n) y[n] = dot2(wp[1][n], op1, dot2(wp[0][n], op0, 0.f));
      const bool b3 = lane & 8, b4 = lane & 16, b5 = lane & 32;
#pragma unroll
      for (int k = 0; k < 4; ++k) { const float keep = b3 ? y[k + 4] : y[k], send = b3 ? y[k] : y[k + 4]; y[k] = keep + dpp<XOR8>(send); }
#pragma unroll
      for (int k = 0; k < 2; ++k) {
        auto s = __builtin_amdgcn_permlane16_swap(__float_as_uint(y[k]), __float_as_uint(y[k + 2]), false, false);
        y[k] = __uint_as_float(s[0]) + __uint_as_float(s[1]);
      }
      {
        auto s = __builtin_amdgcn_permlane32_swap(__float_as_uint(y[0]), __float_as_uint(y[1]), false, false);
        y[0] = __uint_as_float(s[0]) + __uint_as_float(s[1]);
      }
      (void)b4; (void)b5;
      const int n = 8 * p + ((lane >> 3) & 1) * 4 + ((lane >> 4) & 1) * 2 + (lane >> 5);
      LAS float* Y = (LAS float*)(L + L_Y);
      Y[cw * 64 + n] = y[0];
      WAIT_LGKM0();
      int old = 0;
      if (lane == 0) old = __hip_atomic_fetch_add((LAS int*)&C[C_YCNT], 1, __ATOMIC_RELAXED, __HIP_MEMORY_SCOPE_WORKGROUP);
      if (rfl(old) == NC - 1) {
        asm volatile("" ::: "memory");
        if (lane < 32) {
          const f32x2 p0 = *(LAS f32x2*)(Y + 2 * lane), p1 = *(LAS f32x2*)(Y + 64 + 2 * lane), p2 = *(LAS f32x2*)(Y + 128 + 2 * lane), p3 = *(LAS f32x2*)(Y + 192 + 2 * lane);
          const v4u gr = {__float_as_uint((p0[0] + p1[0]) + (p2[0] + p3[0])), tag, __float_as_uint((p0[1] + p1[1]) + (p2[1] + p3[1])), tag};
          __builtin_amdgcn_raw_buffer_store_b128(gr, rs, W_P2 + ((jc * 32 + lane) * 32 + hp) * 16, 0, AUX_SC1);
        }
        STAMP(6);
      }
    }
    return;
  }

  const unsigned tag = __hip_atomic_load((GAS unsigned*)(ws + W_EPOCH), __ATOMIC_RELAXED, __HIP_MEMORY_SCOPE_AGENT) + 1u;
  const v4u xr = *(const GAS v4u*)(x + 8 * lane);
  __builtin_amdgcn_s_barrier();
  bool ok;
  {
    v4u g0, g1;
    auto pass = [&]() -> bool {
      g0 = __builtin_amdgcn_raw_buffer_load_b128(rs, W_QG + G * 2048 + lane * 32, 0, AUX_SC1);
      g1 = __builtin_amdgcn_raw_buffer_load_b128(rs, W_QG + G * 2048 + lane * 32 + 16, 0, AUX_SC1);
      return __all(((g0[1] ^ tag) | (g0[3] ^ tag) | (g1[1] ^ tag) | (g1[3] ^ tag)) == 0u);
    };
    SPIN(pass(), 2, ok);
    if (!ok) { C[C_ABORT] = 1; return; }
    *(LAS v4u*)(L + L_Q + lane * 16) = v4u{g0[0], g0[2], g1[0], g1[2]};
    WAIT_LGKM0();
    C[C_QRDY] = 1;
    STAMP(7);
  }
  float gate0, gate1, x0, x1;
  {
    float xv[8];
#pragma unroll
    for (int k = 0; k < 4; ++k) { xv[2 * k] = bf_lo(xr[k]); xv[2 * k + 1] = bf_hi(xr[k]); }
    float m, den, m2, den2;
    ln_stats(xv, m, den);
#pragma unroll
    for (int k = 0; k < 8; ++k) xv[k] = ln_apply(xv[k], m, den);
    ln_stats(xv, m2, den2);
    const unsigned xd = *(const GAS unsigned*)(x + 2 * b);
    x0 = bf_lo(xd); x1 = bf_hi(xd);
    const float h0 = ln_apply(ln_apply(x0, m, den), m2, den2), h1 = ln_apply(ln_apply(x1, m, den), m2, den2);
    float a0 = 0.f, a1 = 0.f;
    auto gelu = [](float u) {
      const float z2 = -2.f * 0.7978845608028654f * 1.4426950408889634f * fmaf(0.044715f * u * u, u, u);
      return rbf(u * __builtin_amdgcn_rcpf(1.f + __builtin_amdgcn_exp2f(z2)));
    };
#pragma unroll
    for (int i = 0; i < 7; ++i) {
      const int f = 4 * (lane + 64 * i);
      const v2u a = *(const GAS v2u*)(w1 + f), bb = *(const GAS v2u*)(b1 + f), cc = *(const GAS v2u*)(w2 + f);
#pragma unroll
      for (int k = 0; k < 2; ++k) {
        a0 = fmaf(gelu(rbf(rbf(h0 * bf_lo(a[k])) + bf_lo(bb[k]))), bf_lo(cc[k]), a0);
        a0 = fmaf(gelu(rbf(rbf(h0 * bf_hi(a[k])) + bf_hi(bb[k]))), bf_hi(cc[k]), a0);
        a1 = fmaf(gelu(rbf(rbf(h1 * bf_lo(a[k])) + bf_lo(bb[k]))), bf_lo(cc[k]), a1);
        a1 = fmaf(gelu(rbf(rbf(h1 * bf_hi(a[k])) + bf_hi(bb[k]))), bf_hi(cc[k]), a1);
      }
    }
    const float bias2 = bf_lo((unsigned)b2[0]);
    const float y0 = rbf(rbf(wave_sum(a0)) + bias2), y1 = rbf(rbf(wave_sum(a1)) + bias2);
    gate0 = rbf(y0 / (1.f + __expf(-y0)));
    gate1 = rbf(y1 / (1.f + __expf(-y1)));
  }
  STAMP(8);
  SPIN(rfl(C[C_MCNT]) >= NC, 8, ok);
  if (!ok) return;
  {
    v4u g;
    auto pass = [&]() -> bool {
      g = __builtin_amdgcn_raw_buffer_load_b128(rs, W_P2 + (b * 32 + (lane & 31)) * 16, 0, AUX_SC1);
      return __all(((g[1] ^ tag) | (g[3] ^ tag)) == 0u);
    };
    SPIN(pass(), 2, ok);
    if (!ok) return;
    float s0 = __uint_as_float(g[0]), s1 = __uint_as_float(g[2]);
    s0 += dpp<XOR1>(s0); s0 += dpp<XOR2>(s0); s0 += dpp<XOR7>(s0); s0 += dpp<XOR8>(s0); s0 = x16_sum(s0);
    s1 += dpp<XOR1>(s1); s1 += dpp<XOR2>(s1); s1 += dpp<XOR7>(s1); s1 += dpp<XOR8>(s1); s1 = x16_sum(s1);
    if (lane == 0) {
      *(GAS unsigned*)(out + 2 * b) = pk_bf16(x0 + rbf(rbf(s0) * gate0), x1 + rbf(rbf(s1) * gate1));
      if (b == 0) __hip_atomic_store((GAS unsigned*)(ws + W_EPOCH), tag, __ATOMIC_RELAXED, __HIP_MEMORY_SCOPE_AGENT);
    }
    STAMP(9);
    if (b == 0 || PADALL) {
      WAIT_LGKM0();
      const unsigned t0 = TOPSTAMP ? ttop : (unsigned)rfl(C[32]);
      while ((unsigned)__builtin_amdgcn_s_memrealtime() - t0 < 10000u) __builtin_amdgcn_s_sleep(2);
    }
  }
}
__global__ void tiny(unsigned char* ws) { if (threadIdx.x == 1234) ws[1 << 22] = 1; }
}

extern "C" void kernel_launch(void* const* d_in, const int* in_sizes, int n_in, void* d_out, int out_size, void* d_ws, size_t ws_size,
                              hipStream_t stream) {
  (void)hipFuncSetAttribute((const void*)fused_decode, hipFuncAttributeMaxDynamicSharedMemorySize, LDS_BYTES);
  if (PRETINY) hipLaunchKernelGGL(tiny, dim3(1), dim3(64), 0, stream, (unsigned char*)d_ws);
  hipLaunchKernelGGL(fused_decode, dim3(NWG), dim3(NTHR), LDS_BYTES, stream, (const u16*)d_in[0], (const u16*)d_in[1], (const u16*)d_in[2],
                     (const u16*)d_in[3], (const u16*)d_in[4], (const u16*)d_in[6], (const u16*)d_in[7], (const u16*)d_in[8],
                     (const u16*)d_in[9], (u16*)d_out, (unsigned char*)d_ws);
  if (POSTTINY) hipLaunchKernelGGL(tiny, dim3(1), dim3(64), 0, stream, (unsigned char*)d_ws);
}
```

```cpp
#define DIAG 1
#define PADALL 0
#define TOPSTAMP 0
#define PRETINY 1
#define POSTTINY 0
#define VPOL 16
#include <hip/hip_runtime.h>
#include <cstdint>

#define GAS __attribute__((address_space(1)))
#define LAS __attribute__((address_space(3)))
typedef unsigned short u16;
typedef unsigned v4u __attribute__((ext_vector_type(4)));
typedef unsigned v2u __attribute__((ext_vector_type(2)));
typedef float f32x4 __attribute__((ext_vector_type(4)));
typedef float f32x2 __attribute__((ext_vector_type(2)));
typedef short s16x4 __attribute__((ext_vector_type(4)));
typedef __bf16 bf16x8 __attribute__((ext_vector_type(8)));
typedef __bf16 bf16x2 __attribute__((ext_vector_type(2)));

namespace {
constexpr int D = 512, HQ = 32, HKV = 8, SK = 65536, DH = 128, FF = 1792;
constexpr int NWG = 256, NC = 4, NWAVE = NC + 2, NTHR = NWAVE * 64;
constexpr int TK = 32;
constexpr int SLOT = 2 * TK * 256;
constexpr int NSLOT = 9;
constexpr int TPW = SK / 32 / TK;
constexpr int L_RING = 0;
constexpr int L_CTRL = NSLOT * SLOT;
constexpr int C_FULL = 0, C_TOTAL = 1, C_QRDY = 2, C_QCNT = 3, C_MCNT = 4, C_YCNT = 5, C_ABORT = 6, C_FREE = 16;
constexpr int L_Q = L_CTRL + 256;
constexpr int L_QP = L_Q + 1024;
constexpr int L_ML = L_QP + 256;
constexpr int L_Y = L_ML + 128;
constexpr int L_O = L_Y + 1024;
constexpr int LDS_BYTES = L_O + 8192;
static_assert(LDS_BYTES <= 160 * 1024, "LDS");
constexpr unsigned W_EPOCH = 0;
constexpr unsigned W_QG = 4096;
constexpr unsigned W_LSE = W_QG + 8 * 256 * 8;
constexpr unsigned W_P2 = W_LSE + 32 * 32 * 8;
constexpr unsigned W_P1 = W_P2 + 256 * 64 * 8;
constexpr unsigned W_END = W_P1 + 32 * 4 * 32 * 32 * 8;
constexpr unsigned W_STAMP = W_END;
constexpr int AUX_SC1 = 16, AUX_NT = 2;
constexpr unsigned SPIN_CAP = 1u << 22;

__device__ __forceinline__ float bf_lo(unsigned u) { return __uint_as_float(u << 16); }
__device__ __forceinline__ float bf_hi(unsigned u) { return __uint_as_float(u & 0xffff0000u); }
__device__ __forceinline__ unsigned pk_bf16(float lo, float hi) {
  bf16x2 r = {(__bf16)lo, (__bf16)hi};
  return __builtin_bit_cast(unsigned, r);
}
__device__ __forceinline__ float rbf(float f) { return bf_lo(pk_bf16(f, f)); }
template <int CTRL> __device__ __forceinline__ float dpp(float x) {
  return __builtin_bit_cast(float, __builtin_amdgcn_mov_dpp(__builtin_bit_cast(int, x), CTRL, 0xf, 0xf, true));
}
constexpr int XOR1 = 0xB1, XOR2 = 0x4E, XOR7 = 0x141, XOR8 = 0x128;
__device__ __forceinline__ float x16_sum(float x) { auto s = __builtin_amdgcn_permlane16_swap(__float_as_uint(x), __float_as_uint(x), false, false); return __uint_as_float(s[0]) + __uint_as_float(s[1]); }
__device__ __forceinline__ float x32_sum(float x) { auto s = __builtin_amdgcn_permlane32_swap(__float_as_uint(x), __float_as_uint(x), false, false); return __uint_as_float(s[0]) + __uint_as_float(s[1]); }
__device__ __forceinline__ float x16_max(float x) { auto s = __builtin_amdgcn_permlane16_swap(__float_as_uint(x), __float_as_uint(x), false, false); return fmaxf(__uint_as_float(s[0]), __uint_as_float(s[1])); }
__device__ __forceinline__ float x32_max(float x) { auto s = __builtin_amdgcn_permlane32_swap(__float_as_uint(x), __float_as_uint(x), false, false); return fmaxf(__uint_as_float(s[0]), __uint_as_float(s[1])); }
__device__ __forceinline__ float wave_sum(float v) {
  v += dpp<XOR1>(v); v += dpp<XOR2>(v); v += dpp<XOR7>(v); v += dpp<XOR8>(v);
  return x32_sum(x16_sum(v));
}
__device__ __forceinline__ float dot2(unsigned a, unsigned b, float acc) {
  return __builtin_amdgcn_fdot2_f32_bf16(__builtin_bit_cast(bf16x2, a), __builtin_bit_cast(bf16x2, b), acc, false);
}
__device__ __forceinline__ f32x4 mfma16(v4u a, v4u b, f32x4 c) {
  return __builtin_amdgcn_mfma_f32_16x16x32_bf16(__builtin_bit_cast(bf16x8, a), __builtin_bit_cast(bf16x8, b), c, 0, 0, 0);
}
#define GLDS(src, dst, off) __builtin_amdgcn_global_load_lds((const GAS unsigned*)(src), (LAS unsigned*)(dst), 16, (off), AUX_NT)
#define GLDV(src, dst, off) __builtin_amdgcn_global_load_lds((const GAS unsigned*)(src), (LAS unsigned*)(dst), 16, (off), VPOL)
#define WAIT_VM(n) asm volatile("s_waitcnt vmcnt(" #n ")" ::: "memory")
#define WAIT_LGKM0() asm volatile("s_waitcnt lgkmcnt(0)" ::: "memory")
__device__ __forceinline__ int rfl(int v) { return __builtin_amdgcn_readfirstlane(v); }

__device__ __forceinline__ void ln_stats(const float (&v)[8], float& m, float& den) {
  float s = 0.f;
#pragma unroll
  for (int k = 0; k < 8; ++k) s += v[k];
  const float mean = wave_sum(s) * (1.f / D);
  float q = 0.f;
#pragma unroll
  for (int k = 0; k < 8; ++k) { const float d = v[k] - mean; q = fmaf(d, d, q); }
  const float var = wave_sum(q) * (1.f / D);
  m = rbf(mean);
  den = rbf(sqrtf(rbf(rbf(var) + 1e-5f)));
}
__device__ __forceinline__ float ln_apply(float x, float m, float den) { return rbf(rbf(x - m) / den); }

#define SPIN(cond, sleepn, okvar) do { unsigned sp_ = 0; okvar = true; _Pragma("nounroll") for (;;) { asm volatile("" ::: "memory"); if (cond) break; \
    if (sleepn) __builtin_amdgcn_s_sleep(sleepn); \
    if ((++sp_ & 63u) == 0u && (sp_ > SPIN_CAP || rfl(C[C_ABORT]) != 0)) { okvar = false; break; } } asm volatile("" ::: "memory"); } while (0)

#define STAMP(i) do { C[32 + (i)] = (int)(unsigned)__builtin_amdgcn_s_memrealtime(); } while (0)

__global__ void __launch_bounds__(NTHR, 2) fused_decode(const u16* __restrict__ x, const u16* __restrict__ kc, const u16* __restrict__ vc,
                                                         const u16* __restrict__ wq, const u16* __restrict__ wo, const u16* __restrict__ w1,
                                                         const u16* __restrict__ b1, const u16* __restrict__ w2, const u16* __restrict__ b2,
                                                         u16* __restrict__ out, unsigned char* __restrict__ ws) {
  const unsigned ttop = (unsigned)__builtin_amdgcn_s_memrealtime();
  __builtin_amdgcn_sched_barrier(0);
  extern __shared__ __attribute__((aligned(16))) unsigned char smem[];
  LAS unsigned char* L = (LAS unsigned char*)smem;
  volatile LAS int* C = (volatile LAS int*)(L + L_CTRL);
  const int tid = threadIdx.x, lane = tid & 63, wave = rfl(tid >> 6), b = blockIdx.x;
  const int G = b >> 5, gi = b & 31;
  const __amdgpu_buffer_rsrc_t rs = __builtin_amdgcn_make_buffer_rsrc((void*)ws, 0, (int)W_END, 0x00020000);

  if (__builtin_expect(wave == 0, 1)) {
    const unsigned tstart = (unsigned)__builtin_amdgcn_s_memrealtime();
    const GAS unsigned char* kb = (const GAS unsigned char*)kc + ((size_t)G * SK + (size_t)gi * (TPW * TK)) * 256;
    const GAS unsigned char* vb = (const GAS unsigned char*)vc + ((size_t)G * SK + (size_t)gi * (TPW * TK)) * 256;
    const int r4 = lane >> 4, c = lane & 15;
    unsigned ko[4], vo[2];
#pragma unroll
    for (int j = 0; j < 4; ++j) ko[j] = r4 * 256 + ((c ^ (4 * j + r4)) * 16);
#pragma unroll
    for (int j = 0; j < 2; ++j) vo[j] = r4 * 256 + ((c ^ ((4 * j + r4) << 1)) * 16);
    auto issue = [&](int t, int s) {
      const GAS unsigned char* ks = kb + (size_t)t * (TK * 256);
      const GAS unsigned char* vs = vb + (size_t)t * (TK * 256);
      LAS unsigned char* dst = L + s * SLOT;
      GLDS(ks + ko[0], dst, 0); GLDS(ks + ko[1], dst, 1024); GLDS(ks + ko[2], dst, 2048); GLDS(ks + ko[3], dst, 3072);
      GLDS(ks + 4096 + ko[0], dst + 4096, 0); GLDS(ks + 4096 + ko[1], dst + 4096, 1024); GLDS(ks + 4096 + ko[2], dst + 4096, 2048); GLDS(ks + 4096 + ko[3], dst + 4096, 3072);
      GLDV(vs + vo[0], dst + 8192, 0); GLDV(vs + vo[1], dst + 8192, 1024); GLDV(vs + vo[0], dst + 8192, 2048); GLDV(vs + vo[1], dst + 8192, 3072);
      GLDV(vs + 4096 + vo[0], dst + 12288, 0); GLDV(vs + 4096 + vo[1], dst + 12288, 1024); GLDV(vs + 4096 + vo[0], dst + 12288, 2048); GLDV(vs + 4096 + vo[1], dst + 12288, 3072);
    };
    __builtin_amdgcn_s_barrier();
    C[32] = (int)tstart;
    issue(0, 0);
    issue(1, 1);
    issue(2, 2);
    WAIT_VM(32); C[C_FULL] = 1;
    STAMP(12);
    int pub = 1, s = 3;
    bool dead = false;
    for (int t = 3; t < TPW; ++t) {
      if (t >= NSLOT) {
        const int need = t - NSLOT + 1;
        if (rfl(C[C_FREE + s]) < need) {
          if (pub < t - 1) { WAIT_VM(16); pub = t - 1; C[C_FULL] = pub; }
          WAIT_VM(0); pub = t; C[C_FULL] = pub;
          bool ok; SPIN(rfl(C[C_FREE + s]) >= need, 1, ok);
          if (!ok) { dead = true; break; }
        }
        asm volatile("" ::: "memory");
      }
      issue(t, s);
      if (pub < t - 1) { WAIT_VM(32); pub = t - 1; C[C_FULL] = pub; }
      s = (s + 1 == NSLOT) ? 0 : s + 1;
    }
    if (dead) { WAIT_VM(0); C[C_ABORT] = 1; return; }
    WAIT_VM(16); C[C_FULL] = TPW - 1;
    WAIT_VM(0); C[C_FULL] = TPW;
    STAMP(1);
    return;
  }

  if (wave <= NC) {
    const int cw = wave - 1;
    const int jrow = (lane & 3) | ((lane >> 3) << 2), half = (lane >> 2) & 1;
    const int col0 = G * 512 + ((gi & 7) * 4 + (gi >> 3)) * 16;
    v4u wv[4];
#pragma unroll
    for (int i = 0; i < 4; ++i) wv[i] = *(const GAS v4u*)(wq + (size_t)(16 * jrow + 4 * cw + i) * 4096 + col0 + 8 * half);
    const v2u xq = *(const GAS v2u*)(x + 16 * jrow + 4 * cw);
    const v4u xr = *(const GAS v4u*)(x + 8 * lane);
    const unsigned tag = __hip_atomic_load((GAS unsigned*)(ws + W_EPOCH), __ATOMIC_RELAXED, __HIP_MEMORY_SCOPE_AGENT) + 1u;
    if (tid < 64 + 64) { C[lane] = (lane == C_TOTAL) ? TPW : 0; }
    WAIT_LGKM0();
    __builtin_amdgcn_sched_barrier(0);
    __builtin_amdgcn_s_barrier();
    __builtin_amdgcn_sched_barrier(0);
    if (cw == 0) STAMP(10);

    {
      float xv[8];
#pragma unroll
      for (int k = 0; k < 4; ++k) { xv[2 * k] = bf_lo(xr[k]); xv[2 * k + 1] = bf_hi(xr[k]); }
      float m, den;
      ln_stats(xv, m, den);
      float xn[4] = {ln_apply(bf_lo(xq[0]), m, den), ln_apply(bf_hi(xq[0]), m, den), ln_apply(bf_lo(xq[1]), m, den), ln_apply(bf_hi(xq[1]), m, den)};
      float a[8];
#pragma unroll
      for (int k = 0; k < 8; ++k) a[k] = 0.f;
#pragma unroll
      for (int i = 0; i < 4; ++i)
#pragma unroll
        for (int k = 0; k < 4; ++k) { a[2 * k] = fmaf(xn[i], bf_lo(wv[i][k]), a[2 * k]); a[2 * k + 1] = fmaf(xn[i], bf_hi(wv[i][k]), a[2 * k + 1]); }
      const bool b0 = lane & 1, b1_ = lane & 2, b3 = lane & 8;
#pragma unroll
      for (int k = 0; k < 4; ++k) { const float keep = b0 ? a[k + 4] : a[k], send = b0 ? a[k] : a[k + 4]; a[k] = keep + dpp<XOR1>(send); }
#pragma unroll
      for (int k = 0; k < 2; ++k) { const float keep = b1_ ? a[k + 2] : a[k], send = b1_ ? a[k] : a[k + 2]; a[k] = keep + dpp<XOR2>(send); }
      { const float keep = b3 ? a[1] : a[0], send = b3 ? a[0] : a[1]; a[0] = keep + dpp<XOR8>(send); }
      const float qs = x32_sum(x16_sum(a[0]));
      const int qc = half * 8 + (lane & 1) * 4 + ((lane >> 1) & 1) * 2 + ((lane >> 3) & 1);
      LAS float* QP = (LAS float*)(L + L_QP);
      if (lane < 16) QP[cw * 16 + qc] = qs;
      WAIT_LGKM0();
      int old = 0;
      if (lane == 0) old = __hip_atomic_fetch_add((LAS int*)&C[C_QCNT], 1, __ATOMIC_RELAXED, __HIP_MEMORY_SCOPE_WORKGROUP);
      if (rfl(old) == NC - 1) {
        asm volatile("" ::: "memory");
        if (lane < 8) {
          const f32x2 p0 = *(LAS f32x2*)(QP + 2 * lane), p1 = *(LAS f32x2*)(QP + 16 + 2 * lane), p2 = *(LAS f32x2*)(QP + 32 + 2 * lane), p3 = *(LAS f32x2*)(QP + 48 + 2 * lane);
          const unsigned pq = pk_bf16((p0[0] + p1[0]) + (p2[0] + p3[0]), (p0[1] + p1[1]) + (p2[1] + p3[1]));
          __hip_atomic_store((GAS unsigned long long*)(ws + W_QG) + (col0 >> 1) + lane, ((unsigned long long)tag << 32) | pq, __ATOMIC_RELAXED, __HIP_MEMORY_SCOPE_AGENT);
        }
        STAMP(2);
      }
    }
    const int hp = b >> 3, jc = b & 7, r3 = lane >> 3, p = lane & 7;
    v4u wov[4];
#pragma unroll
    for (int k = 0; k < 4; ++k) wov[k] = *(const GAS v4u*)(wo + (size_t)(hp * 128 + 32 * cw + 8 * k + r3) * 512 + 64 * jc + 8 * p);

    const int g4 = lane >> 4, r16 = lane & 15;
    unsigned kaddr[4];
#pragma unroll
    for (int s = 0; s < 4; ++s) kaddr[s] = r16 * 256 + (((4 * s + g4) ^ r16) * 16);
    const int vrow = 4 * g4 + ((lane >> 2) & 3);
    const unsigned vaddr0 = 8192 + vrow * 256 + (vrow & 7) * 32 + (lane & 3) * 8;
    v4u qf[4];
    f32x4 O[8];
#pragma unroll
    for (int d = 0; d < 8; ++d) O[d] = f32x4{0.f, 0.f, 0.f, 0.f};
    float mrun = -INFINITY, lsum = 0.f;
    constexpr float SCALE = 0.08838834764831845f, LOG2E = 1.4426950408889634f;
    bool dead = false;
    int slot = cw;
    for (int t = cw; t < TPW; t += NC) {
      bool ok; SPIN(rfl(C[C_FULL]) > t, 0, ok);
      if (!ok) { dead = true; break; }
      const unsigned sb = slot * SLOT;
      v4u kf[2][4];
      v4u vf[8];
#pragma unroll
      for (int s = 0; s < 4; ++s) {
        kf[0][s] = *(const LAS v4u*)(L + sb + kaddr[s]);
        kf[1][s] = *(const LAS v4u*)(L + sb + kaddr[s] + 4096);
      }
#pragma unroll
      for (int d = 0; d < 8; ++d) {
        const unsigned a = (sb + vaddr0) ^ (d * 32);
        const s16x4 lo = __builtin_amdgcn_ds_read_tr16_b64_v4i16((LAS s16x4*)(L + a));
        const s16x4 hi = __builtin_amdgcn_ds_read_tr16_b64_v4i16((LAS s16x4*)(L + a + 4096));
        const v2u l2 = __builtin_bit_cast(v2u, lo), h2 = __builtin_bit_cast(v2u, hi);
        vf[d] = v4u{l2[0], l2[1], h2[0], h2[1]};
      }
      WAIT_LGKM0();
      C[C_FREE + slot] = t + 1;
      asm volatile("" : "+v"(kf[0][0]), "+v"(kf[0][1]), "+v"(kf[0][2]), "+v"(kf[0][3]), "+v"(kf[1][0]), "+v"(kf[1][1]), "+v"(kf[1][2]), "+v"(kf[1][3]) :: "memory");
      asm volatile("" : "+v"(vf[0]), "+v"(vf[1]), "+v"(vf[2]), "+v"(vf[3]), "+v"(vf[4]), "+v"(vf[5]), "+v"(vf[6]), "+v"(vf[7]) :: "memory");
      slot += NC; if (slot >= NSLOT) slot -= NSLOT;
      if (t == cw) {
        SPIN(rfl(C[C_QRDY]) != 0, 1, ok);
        if (!ok) { dead = true; break; }
#pragma unroll
        for (int s = 0; s < 4; ++s) qf[s] = *(const LAS v4u*)(L + L_Q + (lane & 3) * 256 + s * 64 + g4 * 16);
      }
      f32x4 s0 = {0.f, 0.f, 0.f, 0.f}, s1 = {0.f, 0.f, 0.f, 0.f};
#pragma unroll
      for (int s = 0; s < 4; ++s) { s0 = mfma16(kf[0][s], qf[s], s0); s1 = mfma16(kf[1][s], qf[s], s1); }
      float sc[8];
#pragma unroll
      for (int k = 0; k < 4; k += 2) {
        const unsigned u0 = pk_bf16(s0[k], s0[k + 1]), u1 = pk_bf16(s1[k], s1[k + 1]);
        const unsigned v0 = pk_bf16(bf_lo(u0) * SCALE, bf_hi(u0) * SCALE), v1 = pk_bf16(bf_lo(u1) * SCALE, bf_hi(u1) * SCALE);
        sc[k] = bf_lo(v0) * LOG2E; sc[k + 1] = bf_hi(v0) * LOG2E; sc[4 + k] = bf_lo(v1) * LOG2E; sc[4 + k + 1] = bf_hi(v1) * LOG2E;
      }
      float pm = fmaxf(fmaxf(fmaxf(sc[0], sc[1]), fmaxf(sc[2], sc[3])), fmaxf(fmaxf(sc[4], sc[5]), fmaxf(sc[6], sc[7])));
      if (__any(pm > mrun)) {
        pm = x32_max(x16_max(pm));
        const float mn = fmaxf(mrun, pm);
        const float alpha = __builtin_amdgcn_exp2f(mrun - mn);
        mrun = mn;
        lsum *= alpha;
#pragma unroll
        for (int d = 0; d < 8; ++d) O[d] *= alpha;
      }
      float pr[8];
#pragma unroll
      for (int k = 0; k < 8; ++k) pr[k] = __builtin_amdgcn_exp2f(sc[k] - mrun);
      lsum += ((pr[0] + pr[1]) + (pr[2] + pr[3])) + ((pr[4] + pr[5]) + (pr[6] + pr[7]));
      const v4u pf = {pk_bf16(pr[0], pr[1]), pk_bf16(pr[2], pr[3]), pk_bf16(pr[4], pr[5]), pk_bf16(pr[6], pr[7])};
#pragma unroll
      for (int d = 0; d < 8; ++d) O[d] = mfma16(vf[d], pf, O[d]);
    }
    if (dead) { C[C_ABORT] = 1; return; }
    if (cw == 0) STAMP(3);
    lsum = x32_sum(x16_sum(lsum));
    LAS float* ML = (LAS float*)(L + L_ML);
    LAS float* OB = (LAS float*)(L + L_O);
    if (r16 < 4) {
#pragma unroll
      for (int d = 0; d < 8; ++d) *(LAS f32x4*)(OB + (cw * 4 + r16) * 128 + 16 * d + 4 * g4) = O[d];
      if (g4 == 0) *(LAS f32x2*)(ML + (cw * 4 + r16) * 2) = f32x2{mrun, lsum};
    }
    WAIT_LGKM0();
    if (lane == 0) __hip_atomic_fetch_add((LAS int*)&C[C_MCNT], 1, __ATOMIC_RELAXED, __HIP_MEMORY_SCOPE_WORKGROUP);
    {
      bool ok; SPIN(rfl(C[C_MCNT]) >= NC, 0, ok);
      if (!ok) { C[C_ABORT] = 1; return; }
    }
    {
      const int d0 = (lane >> 4) * 32 + ((lane >> 3) & 1) * 16 + (lane & 7);
      float mw[NC], lw[NC], o0[NC], o1[NC];
#pragma unroll
      for (int w = 0; w < NC; ++w) {
        const f32x2 ml = *(LAS f32x2*)(ML + (w * 4 + cw) * 2);
        mw[w] = ml[0]; lw[w] = ml[1];
        o0[w] = OB[(w * 4 + cw) * 128 + d0]; o1[w] = OB[(w * 4 + cw) * 128 + d0 + 8];
      }
      const float M = fmaxf(fmaxf(mw[0], mw[1]), fmaxf(mw[2], mw[3]));
      float Ls = 0.f, a0 = 0.f, a1 = 0.f;
#pragma unroll
      for (int w = 0; w < NC; ++w) { const float wt = __builtin_amdgcn_exp2f(mw[w] - M); Ls = fmaf(wt, lw[w], Ls); a0 = fmaf(wt, o0[w], a0); a1 = fmaf(wt, o1[w], a1); }
      const float inv = 1.f / Ls;
      const int h = 4 * G + cw;
      const v4u gr = {__float_as_uint(a0 * inv), tag, __float_as_uint(a1 * inv), tag};
      __builtin_amdgcn_raw_buffer_store_b128(gr, rs, W_P1 + ((h * 4 + (lane >> 4)) * 32 + gi) * 256 + (lane & 15) * 16, 0, AUX_SC1);
      if (lane == 0) {
        const v2u lg = {__float_as_uint(M + __builtin_amdgcn_logf(Ls)), tag};
        __builtin_amdgcn_raw_buffer_store_b64(lg, rs, W_LSE + (h * 32 + (gi & 7) * 4 + (gi >> 3)) * 8, 0, AUX_SC1);
      }
    }
    if (cw == 0) STAMP(4);
    unsigned wp[2][8];
#pragma unroll
    for (int kk = 0; kk < 2; ++kk)
#pragma unroll
      for (int k = 0; k < 4; ++k) {
        wp[kk][2 * k] = __builtin_amdgcn_perm(wov[2 * kk + 1][k], wov[2 * kk][k], 0x05040100u);
        wp[kk][2 * k + 1] = __builtin_amdgcn_perm(wov[2 * kk + 1][k], wov[2 * kk][k], 0x07060302u);
      }
    v4u pd[4][2], pl[2];
    {
      const unsigned dbase = W_P1 + ((hp * 4 + cw) * 32 + p) * 256 + r3 * 16;
      const unsigned lbase = W_LSE + (hp * 32 + p * 4) * 8;
      auto pass = [&]() -> bool {
#pragma unroll
        for (int i = 0; i < 4; ++i) {
          pd[i][0] = __builtin_amdgcn_raw_buffer_load_b128(rs, dbase + i * 2048, 0, AUX_SC1);
          pd[i][1] = __builtin_amdgcn_raw_buffer_load_b128(rs, dbase + i * 2048 + 128, 0, AUX_SC1);
        }
        pl[0] = __builtin_amdgcn_raw_buffer_load_b128(rs, lbase, 0, AUX_SC1);
        pl[1] = __builtin_amdgcn_raw_buffer_load_b128(rs, lbase + 16, 0, AUX_SC1);
        unsigned bad = (pl[0][1] ^ tag) | (pl[0][3] ^ tag) | (pl[1][1] ^ tag) | (pl[1][3] ^ tag);
#pragma unroll
        for (int i = 0; i < 4; ++i) bad |= (pd[i][0][1] ^ tag) | (pd[i][0][3] ^ tag) | (pd[i][1][1] ^ tag) | (pd[i][1][3] ^ tag);
        return __all(bad == 0u);
      };
      bool ok; SPIN(pass(), 4, ok);
      if (!ok) { C[C_ABORT] = 1; return; }
    }
    if (cw == 0) STAMP(5);
    {
      const float e0 = __uint_as_float(pl[0][0]), e1 = __uint_as_float(pl[0][2]), e2 = __uint_as_float(pl[1][0]), e3 = __uint_as_float(pl[1][2]);
      float M = fmaxf(fmaxf(e0, e1), fmaxf(e2, e3));
      M = fmaxf(M, dpp<XOR1>(M)); M = fmaxf(M, dpp<XOR2>(M)); M = fmaxf(M, dpp<XOR7>(M));
      const float wt[4] = {__builtin_amdgcn_exp2f(e0 - M), __builtin_amdgcn_exp2f(e1 - M), __builtin_amdgcn_exp2f(e2 - M), __builtin_amdgcn_exp2f(e3 - M)};
      float Ls = (wt[0] + wt[1]) + (wt[2] + wt[3]);
      float o[4] = {0.f, 0.f, 0.f, 0.f};
#pragma unroll
      for (int i = 0; i < 4; ++i) {
        o[0] = fmaf(wt[i], __uint_as_float(pd[i][0][0]), o[0]); o[1] = fmaf(wt[i], __uint_as_float(pd[i][0][2]), o[1]);
        o[2] = fmaf(wt[i], __uint_as_float(pd[i][1][0]), o[2]); o[3] = fmaf(wt[i], __uint_as_float(pd[i][1][2]), o[3]);
      }
      Ls += dpp<XOR1>(Ls); Ls += dpp<XOR2>(Ls); Ls += dpp<XOR7>(Ls);
#pragma unroll
      for (int k = 0; k < 4; ++k) { o[k] += dpp<XOR1>(o[k]); o[k] += dpp<XOR2>(o[k]); o[k] += dpp<XOR7>(o[k]); }
      const float inv = 1.f / Ls;
      const unsigned op0 = pk_bf16(o[0] * inv, o[1] * inv), op1 = pk_bf16(o[2] * inv, o[3] * inv);
      float y[8];
#pragma unroll
      for (int n = 0; n < 8; ++n) y[n] = dot2(wp[1][n], op1, dot2(wp[0][n], op0, 0.f));
      const bool b3 = lane & 8, b4 = lane & 16, b5 = lane & 32;
#pragma unroll
      for (int k = 0; k < 4; ++k) { const float keep = b3 ? y[k + 4] : y[k], send = b3 ? y[k] : y[k + 4]; y[k] = keep + dpp<XOR8>(send); }
#pragma unroll
      for (int k = 0; k < 2; ++k) {
        auto s = __builtin_amdgcn_permlane16_swap(__float_as_uint(y[k]), __float_as_uint(y[k + 2]), false, false);
        y[k] = __uint_as_float(s[0]) + __uint_as_float(s[1]);
      }
      {
        auto s = __builtin_amdgcn_permlane32_swap(__float_as_uint(y[0]), __float_as_uint(y[1]), false, false);
        y[0] = __uint_as_float(s[0]) + __uint_as_float(s[1]);
      }
      (void)b4; (void)b5;
      const int n = 8 * p + ((lane >> 3) & 1) * 4 + ((lane >> 4) & 1) * 2 + (lane >> 5);
      LAS float* Y = (LAS float*)(L + L_Y);
      Y[cw * 64 + n] = y[0];
      WAIT_LGKM0();
      int old = 0;
      if (lane == 0) old = __hip_atomic_fetch_add((LAS int*)&C[C_YCNT], 1, __ATOMIC_RELAXED, __HIP_MEMORY_SCOPE_WORKGROUP);
      if (rfl(old) == NC - 1) {
        asm volatile("" ::: "memory");
        if (lane < 32) {
          const f32x2 p0 = *(LAS f32x2*)(Y + 2 * lane), p1 = *(LAS f32x2*)(Y + 64 + 2 * lane), p2 = *(LAS f32x2*)(Y + 128 + 2 * lane), p3 = *(LAS f32x2*)(Y + 192 + 2 * lane);
          const v4u gr = {__float_as_uint((p0[0] + p1[0]) + (p2[0] + p3[0])), tag, __float_as_uint((p0[1] + p1[1]) + (p2[1] + p3[1])), tag};
          __builtin_amdgcn_raw_buffer_store_b128(gr, rs, W_P2 + ((jc * 32 + lane) * 32 + hp) * 16, 0, AUX_SC1);
        }
        STAMP(6);
      }
    }
    return;
  }

  const unsigned tag = __hip_atomic_load((GAS unsigned*)(ws + W_EPOCH), __ATOMIC_RELAXED, __HIP_MEMORY_SCOPE_AGENT) + 1u;
  const v4u xr = *(const GAS v4u*)(x + 8 * lane);
  __builtin_amdgcn_s_barrier();
  bool ok;
  {
    v4u g0, g1;
    auto pass = [&]() -> bool {
      g0 = __builtin_amdgcn_raw_buffer_load_b128(rs, W_QG + G * 2048 + lane * 32, 0, AUX_SC1);
      g1 = __builtin_amdgcn_raw_buffer_load_b128(rs, W_QG + G * 2048 + lane * 32 + 16, 0, AUX_SC1);
      return __all(((g0[1] ^ tag) | (g0[3] ^ tag) | (g1[1] ^ tag) | (g1[3] ^ tag)) == 0u);
    };
    SPIN(pass(), 2, ok);
    if (!ok) { C[C_ABORT] = 1; return; }
    *(LAS v4u*)(L + L_Q + lane * 16) = v4u{g0[0], g0[2], g1[0], g1[2]};
    WAIT_LGKM0();
    C[C_QRDY] = 1;
    STAMP(7);
  }
  float gate0, gate1, x0, x1;
  {
    float xv[8];
#pragma unroll
    for (int k = 0; k < 4; ++k) { xv[2 * k] = bf_lo(xr[k]); xv[2 * k + 1] = bf_hi(xr[k]); }
    float m, den, m2, den2;
    ln_stats(xv, m, den);
#pragma unroll
    for (int k = 0; k < 8; ++k) xv[k] = ln_apply(xv[k], m, den);
    ln_stats(xv, m2, den2);
    const unsigned xd = *(const GAS unsigned*)(x + 2 * b);
    x0 = bf_lo(xd); x1 = bf_hi(xd);
    const float h0 = ln_apply(ln_apply(x0, m, den), m2, den2), h1 = ln_apply(ln_apply(x1, m, den), m2, den2);
    float a0 = 0.f, a1 = 0.f;
    auto gelu = [](float u) {
      const float z2 = -2.f * 0.7978845608028654f * 1.4426950408889634f * fmaf(0.044715f * u * u, u, u);
      return rbf(u * __builtin_amdgcn_rcpf(1.f + __builtin_amdgcn_exp2f(z2)));
    };
#pragma unroll
    for (int i = 0; i < 7; ++i) {
      const int f = 4 * (lane + 64 * i);
      const v2u a = *(const GAS v2u*)(w1 + f), bb = *(const GAS v2u*)(b1 + f), cc = *(const GAS v2u*)(w2 + f);
#pragma unroll
      for (int k = 0; k < 2; ++k) {
        a0 = fmaf(gelu(rbf(rbf(h0 * bf_lo(a[k])) + bf_lo(bb[k]))), bf_lo(cc[k]), a0);
        a0 = fmaf(gelu(rbf(rbf(h0 * bf_hi(a[k])) + bf_hi(bb[k]))), bf_hi(cc[k]), a0);
        a1 = fmaf(gelu(rbf(rbf(h1 * bf_lo(a[k])) + bf_lo(bb[k]))), bf_lo(cc[k]), a1);
        a1 = fmaf(gelu(rbf(rbf(h1 * bf_hi(a[k])) + bf_hi(bb[k]))), bf_hi(cc[k]), a1);
      }
    }
    const float bias2 = bf_lo((unsigned)b2[0]);
    const float y0 = rbf(rbf(wave_sum(a0)) + bias2), y1 = rbf(rbf(wave_sum(a1)) + bias2);
    gate0 = rbf(y0 / (1.f + __expf(-y0)));
    gate1 = rbf(y1 / (1.f + __expf(-y1)));
  }
  STAMP(8);
  SPIN(rfl(C[C_MCNT]) >= NC, 8, ok);
  if (!ok) return;
  {
    v4u g;
    auto pass = [&]() -> bool {
      g = __builtin_amdgcn_raw_buffer_load_b128(rs, W_P2 + (b * 32 + (lane & 31)) * 16, 0, AUX_SC1);
      return __all(((g[1] ^ tag) | (g[3] ^ tag)) == 0u);
    };
    SPIN(pass(), 2, ok);
    if (!ok) return;
    float s0 = __uint_as_float(g[0]), s1 = __uint_as_float(g[2]);
    s0 += dpp<XOR1>(s0); s0 += dpp<XOR2>(s0); s0 += dpp<XOR7>(s0); s0 += dpp<XOR8>(s0); s0 = x16_sum(s0);
    s1 += dpp<XOR1>(s1); s1 += dpp<XOR2>(s1); s1 += dpp<XOR7>(s1); s1 += dpp<XOR8>(s1); s1 = x16_sum(s1);
    if (lane == 0) {
      *(GAS unsigned*)(out + 2 * b) = pk_bf16(x0 + rbf(rbf(s0) * gate0), x1 + rbf(rbf(s1) * gate1));
      if (b == 0) __hip_atomic_store((GAS unsigned*)(ws + W_EPOCH), tag, __ATOMIC_RELAXED, __HIP_MEMORY_SCOPE_AGENT);
    }
    STAMP(9);
    if (b == 0 || PADALL) {
      WAIT_LGKM0();
      const unsigned t0 = rfl(*(volatile unsigned*)(ws + (1 << 22)));
      while ((unsigned)__builtin_amdgcn_s_memrealtime() - t0 < 10000u) __builtin_amdgcn_s_sleep(2);
    }
  }
}
__global__ void tiny(unsigned char* ws) { if (threadIdx.x == 0) *(unsigned*)(ws + (1 << 22)) = (unsigned)__builtin_amdgcn_s_memrealtime(); }
}

extern "C" void kernel_launch(void* const* d_in, const int* in_sizes, int n_in, void* d_out, int out_size, void* d_ws, size_t ws_size,
                              hipStream_t stream) {
  (void)hipFuncSetAttribute((const void*)fused_decode, hipFuncAttributeMaxDynamicSharedMemorySize, LDS_BYTES);
  if (PRETINY) hipLaunchKernelGGL(tiny, dim3(1), dim3(64), 0, stream, (unsigned char*)d_ws);
  hipLaunchKernelGGL(fused_decode, dim3(NWG), dim3(NTHR), LDS_BYTES, stream, (const u16*)d_in[0], (const u16*)d_in[1], (const u16*)d_in[2],
                     (const u16*)d_in[3], (const u16*)d_in[4], (const u16*)d_in[6], (const u16*)d_in[7], (const u16*)d_in[8],
                     (const u16*)d_in[9], (u16*)d_out, (unsigned char*)d_ws);
  if (POSTTINY) hipLaunchKernelGGL(tiny, dim3(1), dim3(64), 0, stream, (unsigned char*)d_ws);
}
```

```cpp
#include <hip/hip_runtime.h>
#include <cstdint>

#define GAS __attribute__((address_space(1)))
#define LAS __attribute__((address_space(3)))
typedef unsigned short u16;
typedef unsigned v4u __attribute__((ext_vector_type(4)));
typedef unsigned v2u __attribute__((ext_vector_type(2)));
typedef float f32x4 __attribute__((ext_vector_type(4)));
typedef float f32x2 __attribute__((ext_vector_type(2)));
typedef short s16x4 __attribute__((ext_vector_type(4)));
typedef __bf16 bf16x8 __attribute__((ext_vector_type(8)));
typedef __bf16 bf16x2 __attribute__((ext_vector_type(2)));

#define TS0 150
#define TSP 555
#define TSP2 600
#define LMAX 100000
#define TMIN 24
#define QDELAY 250
#define P1AT 3880
#define CODE_BYTES 17280

namespace {
constexpr int D = 512, SK = 65536;
constexpr int NWG = 256, NC = 4, NWAVE = NC + 3, NTHR = NWAVE * 64;
constexpr int TK = 32;
constexpr int TB = TK * 256;
constexpr int NSLOT = 17;
constexpr int NT = 64;
constexpr int L_WQ = (NSLOT - 2) * TB;
constexpr int WO_SLOT = (2 * NT) % NSLOT;
static_assert(WO_SLOT + 1 < NSLOT, "wo slice contiguous");
constexpr int L_S = NSLOT * TB;
constexpr int L_CTRL = L_S + 16384;
constexpr int C_FULL = 0  , C_WQRDY = 2  , C_WORDY = 4  , C_QRDY = 6, C_QCNT = 7, C_MCNT = 8, C_YCNT = 9, C_ABORT = 10, C_LCNT = 11, C_FREE = 16;
constexpr int L_Q = L_CTRL + 256;
constexpr int L_QP = L_Q + 1024;
constexpr int L_ML = L_QP + 256;
constexpr int L_X = L_ML + 128;
constexpr int L_EP = L_X + 1024;
constexpr int L_Y = L_EP + 256;
constexpr int L_PF = L_Y + 2048;
constexpr int LDS_BYTES = L_PF + 256;
static_assert(LDS_BYTES <= 160 * 1024 && C_FREE + NSLOT <= 64, "LDS");
constexpr unsigned W_EPOCH = 0;
constexpr unsigned W_QG = 4096;
constexpr unsigned W_GT = W_QG + 8 * 256 * 8;
constexpr unsigned W_P2 = W_GT + 256 * 8;
constexpr unsigned W_P1 = W_P2 + 32 * 64 * 128;
constexpr unsigned W_END = W_P1 + 32 * 32 * 1024;
constexpr unsigned W_STAMP = W_END;
constexpr unsigned OOB = 0x7ffffff0u;
constexpr int AUX_SC1 = 16, AUX_NT = 2;
constexpr unsigned SPIN_CAP = 1u << 19;

__device__ __forceinline__ float bf_lo(unsigned u) { return __uint_as_float(u << 16); }
__device__ __forceinline__ float bf_hi(unsigned u) { return __uint_as_float(u & 0xffff0000u); }
__device__ __forceinline__ unsigned pk_bf16(float lo, float hi) {
  bf16x2 r = {(__bf16)lo, (__bf16)hi};
  return __builtin_bit_cast(unsigned, r);
}
__device__ __forceinline__ float rbf(float f) { return bf_lo(pk_bf16(f, f)); }
template <int CTRL> __device__ __forceinline__ float dpp(float x) {
  return __builtin_bit_cast(float, __builtin_amdgcn_mov_dpp(__builtin_bit_cast(int, x), CTRL, 0xf, 0xf, true));
}
constexpr int XOR1 = 0xB1, XOR2 = 0x4E, XOR7 = 0x141, XOR8 = 0x128;
__device__ __forceinline__ float x16_sum(float x) { auto s = __builtin_amdgcn_permlane16_swap(__float_as_uint(x), __float_as_uint(x), false, false); return __uint_as_float(s[0]) + __uint_as_float(s[1]); }
__device__ __forceinline__ float x32_sum(float x) { auto s = __builtin_amdgcn_permlane32_swap(__float_as_uint(x), __float_as_uint(x), false, false); return __uint_as_float(s[0]) + __uint_as_float(s[1]); }
__device__ __forceinline__ float x16_max(float x) { auto s = __builtin_amdgcn_permlane16_swap(__float_as_uint(x), __float_as_uint(x), false, false); return fmaxf(__uint_as_float(s[0]), __uint_as_float(s[1])); }
__device__ __forceinline__ float x32_max(float x) { auto s = __builtin_amdgcn_permlane32_swap(__float_as_uint(x), __float_as_uint(x), false, false); return fmaxf(__uint_as_float(s[0]), __uint_as_float(s[1])); }
__device__ __forceinline__ float wave_sum(float v) {
  v += dpp<XOR1>(v); v += dpp<XOR2>(v); v += dpp<XOR7>(v); v += dpp<XOR8>(v);
  return x32_sum(x16_sum(v));
}
__device__ __forceinline__ float dot2(unsigned a, unsigned b, float acc) {
  return __builtin_amdgcn_fdot2_f32_bf16(__builtin_bit_cast(bf16x2, a), __builtin_bit_cast(bf16x2, b), acc, false);
}
__device__ __forceinline__ f32x4 mfma16(v4u a, v4u b, f32x4 c) {
  return __builtin_amdgcn_mfma_f32_16x16x32_bf16(__builtin_bit_cast(bf16x8, a), __builtin_bit_cast(bf16x8, b), c, 0, 0, 0);
}
#define GLDS(src, dst, off, aux) __builtin_amdgcn_global_load_lds((const GAS unsigned*)(src), (LAS unsigned*)(dst), 16, (off), (aux))
#define WAIT_VM(n) asm volatile("s_waitcnt vmcnt(" #n ")" ::: "memory")
#define WAIT_LGKM0() asm volatile("s_waitcnt lgkmcnt(0)" ::: "memory")
__device__ __forceinline__ int rfl(int v) { return __builtin_amdgcn_readfirstlane(v); }
__device__ __forceinline__ unsigned clk32() { return (unsigned)__builtin_amdgcn_s_memrealtime(); }

__device__ __forceinline__ void ln_stats(const float (&v)[8], float& m, float& den) {
  float s = 0.f;
#pragma unroll
  for (int k = 0; k < 8; ++k) s += v[k];
  const float mean = wave_sum(s) * (1.f / D);
  float q = 0.f;
#pragma unroll
  for (int k = 0; k < 8; ++k) { const float d = v[k] - mean; q = fmaf(d, d, q); }
  const float var = wave_sum(q) * (1.f / D);
  m = rbf(mean);
  den = rbf(sqrtf(rbf(rbf(var) + 1e-5f)));
}
__device__ __forceinline__ float ln_apply(float x, float m, float den) { return rbf(rbf(x - m) / den); }

#define SPIN(cond, sleepn, okvar) do { unsigned sp_ = 0; okvar = true; _Pragma("nounroll") for (;;) { asm volatile("" ::: "memory"); if (cond) break; \
    if (sleepn) __builtin_amdgcn_s_sleep(sleepn); \
    if ((++sp_ & 63u) == 0u && (sp_ > SPIN_CAP || rfl(C[C_ABORT]) != 0)) { okvar = false; break; } } asm volatile("" ::: "memory"); } while (0)
#define SPIN_OR_QUIT(cond, sleepn) do { bool ok_; SPIN(cond, sleepn, ok_); if (!ok_) { C[C_ABORT] = 1; return; } } while (0)

#define STAMP(i) do {} while (0)
#define STAMPV(i, v) do {} while (0)
#define NOW() 0ull
#define TAKE(v) do {} while (0)

__global__ void __launch_bounds__(NTHR, 2) fused_decode(const u16* __restrict__ x, const u16* __restrict__ kc, const u16* __restrict__ vc,
                                                         const u16* __restrict__ wq, const u16* __restrict__ wo, const u16* __restrict__ w1,
                                                         const u16* __restrict__ b1, const u16* __restrict__ w2, const u16* __restrict__ b2,
                                                         u16* __restrict__ out, unsigned char* __restrict__ ws) {
  extern __shared__ __attribute__((aligned(16))) unsigned char smem[];
  LAS unsigned char* L = (LAS unsigned char*)smem;
  volatile LAS int* C = (volatile LAS int*)(L + L_CTRL);
  const unsigned long long tstart = NOW();
  const int tid = threadIdx.x, lane = tid & 63, wave = rfl(tid >> 6), b = blockIdx.x;
  const int G = b >> 5, gi = b & 31;
  const int db = G * 8 + (gi & 7), cq = gi >> 3;
  const unsigned tw0 = clk32();
  if (wave == 0) {
    const GAS unsigned char* pc = (const GAS unsigned char*)(__builtin_amdgcn_s_getpc() & ~127ull);
#pragma unroll
    for (int k = 0; k < CODE_BYTES / 8192; ++k) __builtin_amdgcn_global_load_lds((const GAS unsigned*)(pc + k * 8192 + lane * 128), (LAS unsigned*)(L + L_PF), 4, 0, 0);
    if (CODE_BYTES % 8192) __builtin_amdgcn_global_load_lds((const GAS unsigned*)(pc + CODE_BYTES - 8192 + lane * 128), (LAS unsigned*)(L + L_PF), 4, 0, 0);
  }
  asm volatile("" :: "s"(kc), "s"(vc), "s"(wq), "s"(x), "s"(ws));
  if (wave == NWAVE - 1) C[lane] = 0;
  WAIT_LGKM0();
  __builtin_amdgcn_sched_barrier(0);
  __builtin_amdgcn_s_barrier();
  __builtin_amdgcn_sched_barrier(0);
  const int col0 = G * 512 + ((gi & 7) * 4 + (gi >> 3)) * 16;
  const int tile0 = ((((gi & 7) + (gi >> 3) + G) & 7) + 8 * (gi >> 3)) * NT;

  if (__builtin_expect(wave < 2, 1)) {
    const int iv = wave;
    {
      const unsigned wo_ = (unsigned)(((lane & 3) | ((lane >> 3) << 2)) * 16 * 8192 + ((lane >> 2) & 1) * 16);
      const GAS unsigned char* wb = (const GAS unsigned char*)wq + col0 * 2 + (size_t)iv * 8 * 8192;
      if (iv == 0) __builtin_amdgcn_global_load_lds((const GAS unsigned*)(ws + W_EPOCH), (LAS unsigned*)(L + L_EP), 4, 0, 0);
      else GLDS((const GAS unsigned char*)x + lane * 16, L + L_X, 0, 0);
#pragma unroll
      for (int i = 0; i < 8; ++i) GLDS(wb + (size_t)i * 8192 + wo_, L + L_WQ + iv * 8192 + i * 1024, 0, 0);
    }
    __builtin_amdgcn_s_setprio(3);
    const size_t boff = ((size_t)G * SK + (size_t)(tile0 + iv) * TK) * 256;
    const GAS unsigned char* src = (const GAS unsigned char*)kc + boff;
    const int r4 = lane >> 4, c = lane & 15;
    unsigned lo[4];
#pragma unroll
    for (int j = 0; j < 4; ++j) lo[j] = r4 * 256 + ((c ^ (4 * j + r4)) * 16);
    auto issue4 = [&](int s, int h) {
      const GAS unsigned char* p = src + h * 4096;
      LAS unsigned char* dst = L + s * TB + h * 4096;
      GLDS(p + lo[0], dst, 0, AUX_NT); GLDS(p + lo[1], dst, 1024, AUX_NT); GLDS(p + lo[2], dst, 2048, AUX_NT); GLDS(p + lo[3], dst, 3072, AUX_NT);
    };
    issue4(iv, 0); issue4(iv, 1); src += 2 * TB;
    issue4(2 + iv, 0); issue4(2 + iv, 1); src += 2 * TB;
    WAIT_VM(16); C[C_WQRDY + iv] = 1;
    const unsigned long long tfirst = NOW();
    int u = 2, s = 4 + iv, pub = 1, lag = 1;
    unsigned long long stF = 0;
    bool dead = false;
    auto block_on = [&](int word, int need) -> bool {
      const unsigned long long ta = NOW();
      WAIT_VM(0); pub = u; C[C_FULL + iv] = pub;
      bool ok; SPIN(rfl(C[word]) >= need, 1, ok);
      stF += NOW() - ta;
      return ok;
    };
#define PACE_WAIT() do { if (lag == 1) WAIT_VM(8); else WAIT_VM(16); } while (0)
    WAIT_VM(8); C[C_FULL + iv] = 1;
#pragma unroll 1
    for (; u < NT; ++u) {
      const int i = 2 * u + iv;
      if (i >= NSLOT - 2) {
        const int word = i >= NSLOT ? C_FREE + s : C_QCNT, need = i >= NSLOT ? i - NSLOT + 1 : NC;
        if (rfl(C[word]) < need && !block_on(word, need)) { dead = true; break; }
        asm volatile("" ::: "memory");
      }
      if (u == NT / 2) {
        src = (const GAS unsigned char*)vc + boff;
#pragma unroll
        for (int j = 0; j < 4; ++j) lo[j] = r4 * 256 + ((c ^ (((4 * j + r4) & 7) << 1)) * 16);
      }
      const unsigned now = clk32();
      issue4(s, 0);
      {
        const int el = (int)(now - tw0) - TS0;
        const int late = el - ((u * (TSP * 1024 / 10)) >> 10);
        lag = (late > 0 && (late < LMAX || u < TMIN || el > ((u * (TSP2 * 1024 / 10)) >> 10))) ? 2 : 1;
      }
      PACE_WAIT();
      issue4(s, 1);
      src += 2 * TB;
      PACE_WAIT();
      if (pub < u + 1 - lag) { pub = u + 1 - lag; C[C_FULL + iv] = pub; }
      s += 2; if (s >= NSLOT) s -= NSLOT;
    }
#undef PACE_WAIT
    if (!dead && rfl(C[C_FREE + s]) < 2 * NT + iv - NSLOT + 1 && !block_on(C_FREE + s, 2 * NT + iv - NSLOT + 1)) dead = true;
    if (dead) { WAIT_VM(0); C[C_ABORT] = 1; return; }
    asm volatile("" ::: "memory");
    {
      const GAS unsigned char* wb = (const GAS unsigned char*)wo + (size_t)(64 * db + 32 * iv + r4) * 1024 + cq * 256 + c * 16;
#pragma unroll
      for (int i = 0; i < 8; ++i) GLDS(wb + i * 4096, L + (WO_SLOT + iv) * TB + i * 1024, 0, AUX_NT);
    }
    if (pub < NT - 1) { WAIT_VM(16); C[C_FULL + iv] = NT - 1; }
    WAIT_VM(8); C[C_FULL + iv] = NT;
    const unsigned long long tend = NOW();
    WAIT_VM(0);
    C[C_WORDY + iv] = 1;
    if (iv) { STAMPV(1, tend); STAMPV(14, tstart + stF); } else { STAMPV(11, tend); STAMPV(0, tstart); STAMPV(12, tfirst); }
    return;
  }

  const __amdgpu_buffer_rsrc_t rs = __builtin_amdgcn_make_buffer_rsrc((void*)ws, 0, (int)W_END, 0x00020000);
  if (wave < 2 + NC) {
    const int cw = wave - 2;
    unsigned long long tA = 0, tB = 0, tC = 0, tF = 0;
    SPIN_OR_QUIT(rfl(C[C_WQRDY] & C[C_WQRDY + 1]) != 0, 1);
    const unsigned tag = *(const LAS unsigned*)(L + L_EP) + 1u;
    {
      const int jrow = (lane & 3) | ((lane >> 3) << 2), half = (lane >> 2) & 1;
      v4u wv[4];
#pragma unroll
      for (int i = 0; i < 4; ++i) wv[i] = *(const LAS v4u*)(L + L_WQ + (4 * cw + i) * 1024 + lane * 16);
      const v2u xq = *(const LAS v2u*)(L + L_X + (16 * jrow + 4 * cw) * 2);
      const v4u xr = *(const LAS v4u*)(L + L_X + 16 * lane);
      float xv[8];
#pragma unroll
      for (int k = 0; k < 4; ++k) { xv[2 * k] = bf_lo(xr[k]); xv[2 * k + 1] = bf_hi(xr[k]); }
      float m, den;
      ln_stats(xv, m, den);
      float xn[4] = {ln_apply(bf_lo(xq[0]), m, den), ln_apply(bf_hi(xq[0]), m, den), ln_apply(bf_lo(xq[1]), m, den), ln_apply(bf_hi(xq[1]), m, den)};
      float a[8];
#pragma unroll
      for (int k = 0; k < 8; ++k) a[k] = 0.f;
#pragma unroll
      for (int i = 0; i < 4; ++i)
#pragma unroll
        for (int k = 0; k < 4; ++k) { a[2 * k] = fmaf(xn[i], bf_lo(wv[i][k]), a[2 * k]); a[2 * k + 1] = fmaf(xn[i], bf_hi(wv[i][k]), a[2 * k + 1]); }
      const bool b0 = lane & 1, b1_ = lane & 2, b3 = lane & 8;
#pragma unroll
      for (int k = 0; k < 4; ++k) { const float keep = b0 ? a[k + 4] : a[k], send = b0 ? a[k] : a[k + 4]; a[k] = keep + dpp<XOR1>(send); }
#pragma unroll
      for (int k = 0; k < 2; ++k) { const float keep = b1_ ? a[k + 2] : a[k], send = b1_ ? a[k] : a[k + 2]; a[k] = keep + dpp<XOR2>(send); }
      { const float keep = b3 ? a[1] : a[0], send = b3 ? a[0] : a[1]; a[0] = keep + dpp<XOR8>(send); }
      const float qs = x32_sum(x16_sum(a[0]));
      const int qc = half * 8 + (lane & 1) * 4 + ((lane >> 1) & 1) * 2 + ((lane >> 3) & 1);
      LAS float* QP = (LAS float*)(L + L_QP);
      if (lane < 16) QP[cw * 16 + qc] = qs;
      WAIT_LGKM0();
      int old = 0;
      if (lane == 0) old = __hip_atomic_fetch_add((LAS int*)&C[C_QCNT], 1, __ATOMIC_RELAXED, __HIP_MEMORY_SCOPE_WORKGROUP);
      if (rfl(old) == NC - 1) {
        asm volatile("" ::: "memory");
        const int l8 = lane & 7;
        const f32x2 p0 = *(LAS f32x2*)(QP + 2 * l8), p1 = *(LAS f32x2*)(QP + 16 + 2 * l8), p2 = *(LAS f32x2*)(QP + 32 + 2 * l8), p3 = *(LAS f32x2*)(QP + 48 + 2 * l8);
        const v2u pq = {pk_bf16((p0[0] + p1[0]) + (p2[0] + p3[0]), (p0[1] + p1[1]) + (p2[1] + p3[1])), tag};
        __builtin_amdgcn_raw_buffer_store_b64(pq, rs, lane >= 8 ? OOB : W_QG + ((col0 >> 1) + lane) * 8, 0, AUX_SC1);
        STAMP(2);
      }
    }

    const int g4 = lane >> 4, r16 = lane & 15;
    const int fword = C_FULL + (cw & 1);
    LAS unsigned char* SW = L + L_S + cw * 4096 + g4 * 64 + (lane & 3) * 16;
    constexpr float SCALE = 0.08838834764831845f, LOG2E = 1.4426950408889634f;
    int slot = cw;
    float smax = -INFINITY;
    {
      unsigned kaddr[4];
#pragma unroll
      for (int s = 0; s < 4; ++s) kaddr[s] = r16 * 256 + (((4 * s + g4) ^ r16) * 16);
      v4u qf[4];
      auto readK = [&](v4u (&kf)[8], int i) -> bool {
        bool ok; SPIN(rfl(C[fword]) > (i >> 1), 0, ok);
        if (!ok) return false;
        const unsigned sb = slot * TB;
#pragma unroll
        for (int s = 0; s < 4; ++s) { kf[s] = *(const LAS v4u*)(L + sb + kaddr[s]); kf[4 + s] = *(const LAS v4u*)(L + sb + kaddr[s] + 4096); }
        WAIT_LGKM0();
        C[C_FREE + slot] = i + 1;
        asm volatile("" : "+v"(kf[0]), "+v"(kf[1]), "+v"(kf[2]), "+v"(kf[3]), "+v"(kf[4]), "+v"(kf[5]), "+v"(kf[6]), "+v"(kf[7]) :: "memory");
        slot += NC; if (slot >= NSLOT) slot -= NSLOT;
        return true;
      };
      auto scores = [&](const v4u (&kf)[8], int j) {
        f32x4 s0 = {0.f, 0.f, 0.f, 0.f}, s1 = {0.f, 0.f, 0.f, 0.f};
#pragma unroll
        for (int s = 0; s < 4; ++s) { s0 = mfma16(kf[s], qf[s], s0); s1 = mfma16(kf[4 + s], qf[s], s1); }
        v4u sv;
#pragma unroll
        for (int k = 0; k < 2; ++k) {
          const unsigned u0 = pk_bf16(s0[2 * k], s0[2 * k + 1]), u1 = pk_bf16(s1[2 * k], s1[2 * k + 1]);
          sv[k] = pk_bf16(bf_lo(u0) * SCALE, bf_hi(u0) * SCALE); sv[2 + k] = pk_bf16(bf_lo(u1) * SCALE, bf_hi(u1) * SCALE);
        }
        if (r16 < 4) *(LAS v4u*)(SW + j * 256) = sv;
#pragma unroll
        for (int k = 0; k < 4; ++k) smax = fmaxf(smax, fmaxf(bf_lo(sv[k]), bf_hi(sv[k])));
      };
      v4u ka[8], kb[8];
      if (!readK(ka, cw) || !readK(kb, cw + NC)) { C[C_ABORT] = 1; return; }
      SPIN_OR_QUIT(rfl(C[C_QRDY]) != 0, 1);
      if (cw == 0) STAMP(10);
#pragma unroll
      for (int s = 0; s < 4; ++s) qf[s] = *(const LAS v4u*)(L + L_Q + (lane & 3) * 256 + s * 64 + g4 * 16);
      scores(ka, 0);
#pragma unroll 1
      for (int j = 1;; ++j) {
        scores(kb, j);
        if (j == NT / NC - 1) break;
        if (!readK(kb, cw + NC * (j + 1))) { C[C_ABORT] = 1; return; }
      }
    }
    const float mw = x32_max(x16_max(smax)) * LOG2E;
    f32x4 O[8];
#pragma unroll
    for (int d = 0; d < 8; ++d) O[d] = f32x4{0.f, 0.f, 0.f, 0.f};
    float wt[NC];
    unsigned lse = 0u;
    {
      const int vrow = 4 * g4 + ((lane >> 2) & 3);
      const unsigned vaddr0 = vrow * 256 + (vrow & 7) * 32 + (lane & 3) * 8;
      float lsum = 0.f;
#pragma unroll 1
      for (int j = 0; j < NT / NC; ++j) {
        const int i = NT + cw + NC * j;
        const v4u sv = *(const LAS v4u*)(SW + j * 256);
        float pr[8];
#pragma unroll
        for (int k = 0; k < 4; ++k) { pr[2 * k] = __builtin_amdgcn_exp2f(fmaf(bf_lo(sv[k]), LOG2E, -mw)); pr[2 * k + 1] = __builtin_amdgcn_exp2f(fmaf(bf_hi(sv[k]), LOG2E, -mw)); }
        lsum += ((pr[0] + pr[1]) + (pr[2] + pr[3])) + ((pr[4] + pr[5]) + (pr[6] + pr[7]));
        v4u pf = {pk_bf16(pr[0], pr[1]), pk_bf16(pr[2], pr[3]), pk_bf16(pr[4], pr[5]), pk_bf16(pr[6], pr[7])};
        asm volatile("" : "+v"(pf));
        if (j == NT / NC - 1) {
          const float ltot = x32_sum(x16_sum(lsum));
          if (lane < 4) *(LAS f32x2*)(L + L_ML + (lane * 4 + cw) * 8) = f32x2{mw, ltot};
          WAIT_LGKM0();
          if (lane == 0) __hip_atomic_fetch_add((LAS int*)&C[C_LCNT], 1, __ATOMIC_RELAXED, __HIP_MEMORY_SCOPE_WORKGROUP);
          SPIN_OR_QUIT(rfl(C[C_LCNT]) >= NC, 0);
          const f32x4 a = *(LAS f32x4*)(L + L_ML + cw * 32), c2 = *(LAS f32x4*)(L + L_ML + cw * 32 + 16);
          const float M = fmaxf(fmaxf(a[0], a[2]), fmaxf(c2[0], c2[2]));
          wt[0] = __builtin_amdgcn_exp2f(a[0] - M); wt[1] = __builtin_amdgcn_exp2f(a[2] - M); wt[2] = __builtin_amdgcn_exp2f(c2[0] - M); wt[3] = __builtin_amdgcn_exp2f(c2[2] - M);
          const float Ls = fmaf(wt[0], a[1], fmaf(wt[1], a[3], fmaf(wt[2], c2[1], wt[3] * c2[3])));
          const float inv = __builtin_amdgcn_rcpf(Ls);
#pragma unroll
          for (int w = 0; w < NC; ++w) wt[w] *= inv;
          lse = __float_as_uint(M + __builtin_amdgcn_logf(Ls));
          asm volatile("" : "+v"(wt[0]), "+v"(wt[1]), "+v"(wt[2]), "+v"(wt[3]), "+v"(lse));
        }
        SPIN_OR_QUIT(rfl(C[fword]) > (i >> 1), 0);
        tF = NOW();
        v4u vf[8];
        const unsigned sb = slot * TB;
#pragma unroll
        for (int d = 0; d < 8; ++d) {
          const unsigned a = (sb + vaddr0) ^ (d * 32);
          const s16x4 lo = __builtin_amdgcn_ds_read_tr16_b64_v4i16((LAS s16x4*)(L + a));
          const s16x4 hi = __builtin_amdgcn_ds_read_tr16_b64_v4i16((LAS s16x4*)(L + a + 4096));
          const v2u l2 = __builtin_bit_cast(v2u, lo), h2 = __builtin_bit_cast(v2u, hi);
          vf[d] = v4u{l2[0], l2[1], h2[0], h2[1]};
        }
#pragma unroll
        for (int d = 0; d < 8; ++d) O[d] = mfma16(vf[d], pf, O[d]);
        WAIT_LGKM0();
        C[C_FREE + slot] = i + 1;
        slot += NC; if (slot >= NSLOT) slot -= NSLOT;
      }
    }
    TAKE(tA);
    {
      if (r16 < 4) {
#pragma unroll
        for (int d = 0; d < 8; ++d) *(LAS f32x4*)(L + L_S + cw * 4096 + (r16 * 128 + 16 * d + 4 * g4) * 4) = O[d];
      }
      WAIT_LGKM0();
      if (lane == 0) __hip_atomic_fetch_add((LAS int*)&C[C_MCNT], 1, __ATOMIC_RELAXED, __HIP_MEMORY_SCOPE_WORKGROUP);
      SPIN_OR_QUIT(rfl(C[C_MCNT]) >= NC, 0);
      float a0 = 0.f, a1 = 0.f;
#pragma unroll
      for (int w = 0; w < NC; ++w) { const f32x2 ow = *(LAS f32x2*)(L + L_S + w * 4096 + (cw * 128 + 2 * lane) * 4); a0 = fmaf(wt[w], ow[0], a0); a1 = fmaf(wt[w], ow[1], a1); }
      const v4u gr = {__float_as_uint(a0), (tag << 16) | (lse >> 16), __float_as_uint(a1), (tag << 16) | (lse & 0xffffu)};
      __builtin_amdgcn_raw_buffer_store_b128(gr, rs, W_P1 + ((4 * G + cw) * 32 + gi) * 1024 + lane * 16, 0, AUX_SC1);
    }
    TAKE(tB);
    v4u pd[4];
    unsigned wp[8][2];
    {
      SPIN_OR_QUIT(rfl(C[C_WORDY + (cw >> 1)]) != 0, 0);
      const LAS unsigned* wsrc = (const LAS unsigned*)(L + WO_SLOT * TB + cw * 4096) + lane;
#pragma unroll
      for (int j = 0; j < 8; ++j) {
        const unsigned wa = wsrc[(2 * j) * 64], wb = wsrc[(2 * j + 1) * 64];
        wp[j][0] = __builtin_amdgcn_perm(wb, wa, 0x05040100u);
        wp[j][1] = __builtin_amdgcn_perm(wb, wa, 0x07060302u);
      }
      const unsigned t16 = tag & 0xffffu;
      unsigned po[4];
#pragma unroll
      for (int i = 0; i < 4; ++i) { po[i] = W_P1 + ((db >> 1) * 32 + 8 * i + (lane >> 3)) * 1024 + ((db & 1) * 4 + cw) * 128 + (lane & 7) * 16; pd[i] = v4u{0u, 0u, 0u, 0u}; }
      if (P1AT) { while (clk32() - tw0 < P1AT) __builtin_amdgcn_s_sleep(1); }
      auto pass = [&]() -> bool {
        v4u n[4];
#pragma unroll
        for (int i = 0; i < 4; ++i) n[i] = __builtin_amdgcn_raw_buffer_load_b128(rs, po[i], 0, AUX_SC1);
#pragma unroll
        for (int i = 0; i < 4; ++i) {
          if (po[i] != OOB) pd[i] = n[i];
          if ((n[i][1] >> 16) == t16 && (n[i][3] >> 16) == t16) po[i] = OOB;
        }
        return __all((po[0] & po[1] & po[2] & po[3]) == OOB);
      };
      SPIN_OR_QUIT(pass(), 0);
    }
    TAKE(tC);
    {
      float e[4];
#pragma unroll
      for (int i = 0; i < 4; ++i) e[i] = __uint_as_float(__builtin_amdgcn_perm(pd[i][1], pd[i][3], 0x05040100u));
      float M = fmaxf(fmaxf(e[0], e[1]), fmaxf(e[2], e[3]));
      M = fmaxf(M, dpp<XOR8>(M)); M = x32_max(x16_max(M));
      float Ls = 0.f, oa = 0.f, ob = 0.f;
#pragma unroll
      for (int i = 0; i < 4; ++i) { const float w = __builtin_amdgcn_exp2f(e[i] - M); Ls += w; oa = fmaf(w, __uint_as_float(pd[i][0]), oa); ob = fmaf(w, __uint_as_float(pd[i][2]), ob); }
      Ls += dpp<XOR8>(Ls); oa += dpp<XOR8>(oa); ob += dpp<XOR8>(ob);
      Ls = x32_sum(x16_sum(Ls)); oa = x32_sum(x16_sum(oa)); ob = x32_sum(x16_sum(ob));
      const float inv = __builtin_amdgcn_rcpf(Ls);
      const unsigned op = pk_bf16(oa * inv, ob * inv);
      float ya = 0.f, yb = 0.f, yc = 0.f, yd = 0.f;
#pragma unroll
      for (int j = 0; j < 8; j += 2) {
        const unsigned o0 = (unsigned)__builtin_amdgcn_readlane((int)op, j), o1 = (unsigned)__builtin_amdgcn_readlane((int)op, j + 1);
        ya = dot2(wp[j][0], o0, ya); yb = dot2(wp[j][1], o0, yb); yc = dot2(wp[j + 1][0], o1, yc); yd = dot2(wp[j + 1][1], o1, yd);
      }
      LAS f32x2* Y = (LAS f32x2*)(L + L_Y);
      Y[cw * 64 + lane] = f32x2{ya + yc, yb + yd};
      WAIT_LGKM0();
      int old = 0;
      if (lane == 0) old = __hip_atomic_fetch_add((LAS int*)&C[C_YCNT], 1, __ATOMIC_RELAXED, __HIP_MEMORY_SCOPE_WORKGROUP);
      if (rfl(old) == NC - 1) {
        asm volatile("" ::: "memory");
        const f32x2 ys = (Y[lane] + Y[64 + lane]) + (Y[128 + lane] + Y[192 + lane]);
        const v4u ga = {__float_as_uint(ys[0]), tag, __float_as_uint(ys[1]), tag};
        __builtin_amdgcn_raw_buffer_store_b128(ga, rs, W_P2 + ((cq * 8 + (lane >> 3)) * 64 + db) * 128 + (lane & 7) * 16, 0, AUX_SC1);
        STAMP(6);
      }
    }
    STAMPV(16 + cw, tA); STAMPV(20 + cw, tB); STAMPV(24 + cw, tC); STAMPV(28 + cw, tF);
    return;
  }

  SPIN_OR_QUIT(rfl(C[C_WQRDY] & C[C_WQRDY + 1]) != 0, 1);
  const unsigned tag = *(const LAS unsigned*)(L + L_EP) + 1u;
  {
    { const unsigned tq = clk32(); while (clk32() - tq < QDELAY) __builtin_amdgcn_s_sleep(2); }
    v4u g0, g1;
    auto pass = [&]() -> bool {
      g0 = __builtin_amdgcn_raw_buffer_load_b128(rs, W_QG + G * 2048 + lane * 32, 0, AUX_SC1);
      g1 = __builtin_amdgcn_raw_buffer_load_b128(rs, W_QG + G * 2048 + lane * 32 + 16, 0, AUX_SC1);
      return __all(((g0[1] ^ tag) | (g0[3] ^ tag) | (g1[1] ^ tag) | (g1[3] ^ tag)) == 0u);
    };
    SPIN_OR_QUIT(pass(), 1);
    *(LAS v4u*)(L + L_Q + lane * 16) = v4u{g0[0], g0[2], g1[0], g1[2]};
    WAIT_LGKM0();
    C[C_QRDY] = 1;
    STAMP(7);
  }
  {
    const v4u xr = *(const LAS v4u*)(L + L_X + 16 * lane);
    float xv[8];
#pragma unroll
    for (int k = 0; k < 4; ++k) { xv[2 * k] = bf_lo(xr[k]); xv[2 * k + 1] = bf_hi(xr[k]); }
    float m, den, m2, den2;
    ln_stats(xv, m, den);
#pragma unroll
    for (int k = 0; k < 8; ++k) xv[k] = ln_apply(xv[k], m, den);
    ln_stats(xv, m2, den2);
    const unsigned xd = *(const LAS unsigned*)(L + L_X + 4 * b);
    const float h0 = ln_apply(ln_apply(bf_lo(xd), m, den), m2, den2), h1 = ln_apply(ln_apply(bf_hi(xd), m, den), m2, den2);
    float a0 = 0.f, a1 = 0.f;
    auto gelu = [](float u) {
      const float z2 = -2.f * 0.7978845608028654f * 1.4426950408889634f * fmaf(0.044715f * u * u, u, u);
      return rbf(u * __builtin_amdgcn_rcpf(1.f + __builtin_amdgcn_exp2f(z2)));
    };
    v2u na = *(const GAS v2u*)(w1 + 4 * lane), nb = *(const GAS v2u*)(b1 + 4 * lane), nc = *(const GAS v2u*)(w2 + 4 * lane);
#pragma unroll 1
    for (int i = 1; i <= 7; ++i) {
      const v2u a = na, bb = nb, cc = nc;
      const int f = 4 * (lane + 64 * (i < 7 ? i : 0));
      na = *(const GAS v2u*)(w1 + f); nb = *(const GAS v2u*)(b1 + f); nc = *(const GAS v2u*)(w2 + f);
#pragma unroll
      for (int k = 0; k < 2; ++k) {
        a0 = fmaf(gelu(rbf(rbf(h0 * bf_lo(a[k])) + bf_lo(bb[k]))), bf_lo(cc[k]), a0);
        a0 = fmaf(gelu(rbf(rbf(h0 * bf_hi(a[k])) + bf_hi(bb[k]))), bf_hi(cc[k]), a0);
        a1 = fmaf(gelu(rbf(rbf(h1 * bf_lo(a[k])) + bf_lo(bb[k]))), bf_lo(cc[k]), a1);
        a1 = fmaf(gelu(rbf(rbf(h1 * bf_hi(a[k])) + bf_hi(bb[k]))), bf_hi(cc[k]), a1);
      }
    }
    const float bias2 = bf_lo((unsigned)b2[0]);
    const float y0 = rbf(rbf(wave_sum(a0)) + bias2), y1 = rbf(rbf(wave_sum(a1)) + bias2);
    const unsigned gates = pk_bf16(y0 / (1.f + __expf(-y0)), y1 / (1.f + __expf(-y1)));
    if (lane == 0) __hip_atomic_store((GAS unsigned long long*)(ws + W_GT) + b, ((unsigned long long)tag << 32) | gates, __ATOMIC_RELAXED, __HIP_MEMORY_SCOPE_AGENT);
  }
  STAMP(8);
  if ((b & 7) != 0) return;
  const int r = b >> 3;
  SPIN_OR_QUIT(rfl(C[C_MCNT]) >= NC, 4);
  unsigned gates;
  {
    unsigned long long gg;
    auto pass = [&]() -> bool {
      gg = __hip_atomic_load((GAS unsigned long long*)(ws + W_GT) + b + (lane & 7), __ATOMIC_RELAXED, __HIP_MEMORY_SCOPE_AGENT);
      return __all((unsigned)(gg >> 32) == tag);
    };
    SPIN_OR_QUIT(pass(), 1);
    gates = (unsigned)gg;
  }
  const unsigned xo = *(const LAS unsigned*)(L + L_X + 32 * r + 4 * (lane & 7));
  const __amdgpu_buffer_rsrc_t ors = __builtin_amdgcn_make_buffer_rsrc((void*)out, 0, D * 2, 0x00020000);
  {
    v4u g[8];
    unsigned po[8];
#pragma unroll
    for (int i = 0; i < 8; ++i) { po[i] = W_P2 + r * 8192 + i * 1024 + lane * 16; g[i] = v4u{0u, 0u, 0u, 0u}; }
    auto pass = [&]() -> bool {
      v4u n[8];
#pragma unroll
      for (int i = 0; i < 8; ++i) n[i] = __builtin_amdgcn_raw_buffer_load_b128(rs, po[i], 0, AUX_SC1);
      __builtin_amdgcn_sched_barrier(0);
      unsigned all = OOB;
#pragma unroll
      for (int i = 0; i < 8; ++i) {
        if (po[i] != OOB) g[i] = n[i];
        if (n[i][1] == tag && n[i][3] == tag) po[i] = OOB;
        all &= po[i];
      }
      return __all(all == OOB);
    };
    SPIN_OR_QUIT(pass(), 0);
    float s0 = 0.f, s1 = 0.f;
#pragma unroll
    for (int i = 0; i < 8; ++i) { s0 += __uint_as_float(g[i][0]); s1 += __uint_as_float(g[i][2]); }
    s0 += dpp<XOR8>(s0); s1 += dpp<XOR8>(s1);
    s0 = x32_sum(x16_sum(s0)); s1 = x32_sum(x16_sum(s1));
    const unsigned res = pk_bf16(bf_lo(xo) + rbf(rbf(s0) * bf_lo(gates)), bf_hi(xo) + rbf(rbf(s1) * bf_hi(gates)));
    __builtin_amdgcn_raw_buffer_store_b32(res, ors, lane >= 8 ? OOB : 32 * r + 4 * lane, 0, 0);
    __builtin_amdgcn_raw_buffer_store_b32(tag, rs, (b != 0 || lane != 0) ? OOB : W_EPOCH, 0, AUX_SC1);
    STAMP(9);
  }
}
}

extern "C" void kernel_launch(void* const* d_in, const int* in_sizes, int n_in, void* d_out, int out_size, void* d_ws, size_t ws_size,
                              hipStream_t stream) {
  (void)hipFuncSetAttribute((const void*)fused_decode, hipFuncAttributeMaxDynamicSharedMemorySize, LDS_BYTES);
  hipLaunchKernelGGL(fused_decode, dim3(NWG), dim3(NTHR), LDS_BYTES, stream, (const u16*)d_in[0], (const u16*)d_in[1], (const u16*)d_in[2],
                     (const u16*)d_in[3], (const u16*)d_in[4], (const u16*)d_in[6], (const u16*)d_in[7], (const u16*)d_in[8],
                     (const u16*)d_in[9], (u16*)d_out, (unsigned char*)d_ws);
}
```

```cpp
#include <hip/hip_runtime.h>
#include <cstdint>

#define GAS __attribute__((address_space(1)))
#define LAS __attribute__((address_space(3)))
typedef unsigned short u16;
typedef unsigned v4u __attribute__((ext_vector_type(4)));
typedef unsigned v2u __attribute__((ext_vector_type(2)));
typedef float f32x4 __attribute__((ext_vector_type(4)));
typedef float f32x2 __attribute__((ext_vector_type(2)));
typedef short s16x4 __attribute__((ext_vector_type(4)));
typedef __bf16 bf16x8 __attribute__((ext_vector_type(8)));
typedef __bf16 bf16x2 __attribute__((ext_vector_type(2)));

#define CODE_BYTES 22016

namespace {
constexpr int TS0 = 150, TSP = 552;
constexpr int ANCH0 = 190;
constexpr int AHD = 30;
constexpr unsigned QDELAY = 200;
constexpr int NCK = 2, CK0 = 1900, CKD = 1500, CKW = 150, CKTH = 22, CKM = 8, GXT = 20;
constexpr int P1D = 175;
constexpr unsigned P2D = 12;
constexpr int PFN = 8;
constexpr int D = 512, SK = 65536;
constexpr int NWG = 256, NC = 4, NWAVE = NC + 3, NTHR = NWAVE * 64;
constexpr int TK = 32;
constexpr int TB = TK * 256;
constexpr int NSLOT = 17;
constexpr int NT = 64;
constexpr int L_WQ = (NSLOT - 2) * TB;
constexpr int WO_SLOT = (2 * NT) % NSLOT;
static_assert(WO_SLOT + 1 < NSLOT, "wo slice contiguous");
constexpr int L_S = NSLOT * TB;
constexpr int L_CTRL = L_S + 16384;
constexpr int C_FULL = 0  , C_WQRDY = 2  , C_WORDY = 4  , C_QRDY = 6, C_QCNT = 7, C_MCNT = 8, C_YCNT = 9,
              C_ABORT = 10, C_LCNT = 11, C_XCNT = 12, C_DUE = 13, C_ANCH = 14  , C_FREE = 16  ,
              C_GX = 33, C_LATE = 34  , C_VP = 36  ;
static_assert(C_FREE + NSLOT <= C_GX, "control words");
constexpr int L_Q = L_CTRL + 256;
constexpr int L_QP = L_Q + 1024;
constexpr int L_ML = L_QP + 1024;
constexpr int L_X = L_ML + 128;
constexpr int L_EP = L_X + 1024;
constexpr int L_Y = L_EP + 256;
constexpr int L_PF = L_Y + 2048;
constexpr int LDS_BYTES = L_PF + 256;
static_assert(LDS_BYTES <= 160 * 1024 && C_FREE + NSLOT <= 64, "LDS");
constexpr unsigned W_EPOCH = 0;
constexpr unsigned W_QG = 4096;
constexpr unsigned W_TG = W_QG + 8 * 4 * 512 * 8;
constexpr unsigned W_CK = W_TG + 256 * 8;
constexpr unsigned W_GT = W_CK + 4 * 64 * 8;
constexpr unsigned W_P2 = W_GT + 256 * 8;
constexpr unsigned W_P1 = W_P2 + 32 * 64 * 128;
constexpr unsigned W_END = W_P1 + 32 * 32 * 1024;
constexpr unsigned OOB = 0x7ffffff0u;
constexpr int AUX_SC1 = 16, AUX_NT = 2;
constexpr unsigned SPIN_CAP = 1u << 19;

__device__ __forceinline__ float bf_lo(unsigned u) { return __uint_as_float(u << 16); }
__device__ __forceinline__ float bf_hi(unsigned u) { return __uint_as_float(u & 0xffff0000u); }
__device__ __forceinline__ unsigned pk_bf16(float lo, float hi) {
  bf16x2 r = {(__bf16)lo, (__bf16)hi};
  return __builtin_bit_cast(unsigned, r);
}
__device__ __forceinline__ float rbf(float f) { return bf_lo(pk_bf16(f, f)); }
template <int CTRL> __device__ __forceinline__ float dpp(float x) {
  return __builtin_bit_cast(float, __builtin_amdgcn_mov_dpp(__builtin_bit_cast(int, x), CTRL, 0xf, 0xf, true));
}
constexpr int XOR1 = 0xB1, XOR2 = 0x4E, XOR7 = 0x141, XOR8 = 0x128, ROR4 = 0x124;
__device__ __forceinline__ float x16_sum(float x) { auto s = __builtin_amdgcn_permlane16_swap(__float_as_uint(x), __float_as_uint(x), false, false); return __uint_as_float(s[0]) + __uint_as_float(s[1]); }
__device__ __forceinline__ float x32_sum(float x) { auto s = __builtin_amdgcn_permlane32_swap(__float_as_uint(x), __float_as_uint(x), false, false); return __uint_as_float(s[0]) + __uint_as_float(s[1]); }
__device__ __forceinline__ float x16_max(float x) { auto s = __builtin_amdgcn_permlane16_swap(__float_as_uint(x), __float_as_uint(x), false, false); return fmaxf(__uint_as_float(s[0]), __uint_as_float(s[1])); }
__device__ __forceinline__ float x32_max(float x) { auto s = __builtin_amdgcn_permlane32_swap(__float_as_uint(x), __float_as_uint(x), false, false); return fmaxf(__uint_as_float(s[0]), __uint_as_float(s[1])); }
__device__ __forceinline__ float wave_sum(float v) {
  v += dpp<XOR1>(v); v += dpp<XOR2>(v); v += dpp<XOR7>(v); v += dpp<XOR8>(v);
  return x32_sum(x16_sum(v));
}
__device__ __forceinline__ float dot2(unsigned a, unsigned b, float acc) {
  return __builtin_amdgcn_fdot2_f32_bf16(__builtin_bit_cast(bf16x2, a), __builtin_bit_cast(bf16x2, b), acc, false);
}
__device__ __forceinline__ f32x4 mfma16(v4u a, v4u b, f32x4 c) {
  return __builtin_amdgcn_mfma_f32_16x16x32_bf16(__builtin_bit_cast(bf16x8, a), __builtin_bit_cast(bf16x8, b), c, 0, 0, 0);
}
#define GLDS(src, dst, off, aux) __builtin_amdgcn_global_load_lds((const GAS unsigned*)(src), (LAS unsigned*)(dst), 16, (off), (aux))
#define WAIT_VM(n) asm volatile("s_waitcnt vmcnt(" #n ")" ::: "memory")
#define WAIT_LGKM0() asm volatile("s_waitcnt lgkmcnt(0)" ::: "memory")
__device__ __forceinline__ int rfl(int v) { return __builtin_amdgcn_readfirstlane(v); }
__device__ __forceinline__ unsigned clk32() { return (unsigned)__builtin_amdgcn_s_memrealtime(); }

__device__ __forceinline__ void ln_stats(const float (&v)[8], float& m, float& den) {
  float s = 0.f;
#pragma unroll
  for (int k = 0; k < 8; ++k) s += v[k];
  const float mean = wave_sum(s) * (1.f / D);
  float q = 0.f;
#pragma unroll
  for (int k = 0; k < 8; ++k) { const float d = v[k] - mean; q = fmaf(d, d, q); }
  const float var = wave_sum(q) * (1.f / D);
  m = rbf(mean);
  den = rbf(sqrtf(rbf(rbf(var) + 1e-5f)));
}
__device__ __forceinline__ float ln_apply(float x, float m, float den) { return rbf(rbf(x - m) / den); }

#define SPIN(cond, sleepn, okvar) do { unsigned sp_ = 0; okvar = true; _Pragma("nounroll") for (;;) { asm volatile("" ::: "memory"); if (cond) break; \
    if (sleepn) __builtin_amdgcn_s_sleep(sleepn); \
    if ((++sp_ & 63u) == 0u && (sp_ > SPIN_CAP || rfl(C[C_ABORT]) != 0)) { okvar = false; break; } } asm volatile("" ::: "memory"); } while (0)
#define SPIN_OR_QUIT(cond, sleepn) do { bool ok_; SPIN(cond, sleepn, ok_); if (!ok_) { C[C_ABORT] = 1; return; } } while (0)

__global__ void __launch_bounds__(NTHR, 2) fused_decode(const u16* __restrict__ x, const u16* __restrict__ kc, const u16* __restrict__ vc,
                                                         const u16* __restrict__ wq, const u16* __restrict__ wo, const u16* __restrict__ w1,
                                                         const u16* __restrict__ b1, const u16* __restrict__ w2, const u16* __restrict__ b2,
                                                         u16* __restrict__ out, unsigned char* __restrict__ ws) {
  extern __shared__ __attribute__((aligned(16))) unsigned char smem[];
  LAS unsigned char* L = (LAS unsigned char*)smem;
  volatile LAS int* C = (volatile LAS int*)(L + L_CTRL);
  const int tid = threadIdx.x, lane = tid & 63, wave = rfl(tid >> 6), b = blockIdx.x;
  const int G = b >> 5, gi = b & 31;
  const int db = G * 8 + (gi & 7), cq = gi >> 3;
  const unsigned tw0 = clk32();
  constexpr int NL = CODE_BYTES / 128, PER = (NL + PFN - 1) / PFN;
  static_assert(PER <= 64, "one instruction");
  const GAS unsigned char* pcb = (const GAS unsigned char*)(__builtin_amdgcn_s_getpc() & ~127ull);
  const int pfl = min(((b >> 3) & (PFN - 1)) + PFN * min(lane, PER - 1), NL - 1);
  if (wave == 0) __builtin_amdgcn_global_load_lds((const GAS unsigned*)(pcb + pfl * 128), (LAS unsigned*)(L + L_PF), 4, 0, 0);
  asm volatile("" :: "s"(kc), "s"(vc), "s"(wq), "s"(x), "s"(ws));
  if (wave == NWAVE - 1) C[lane] = 0;
  WAIT_LGKM0();
  __builtin_amdgcn_sched_barrier(0);
  __builtin_amdgcn_s_barrier();
  __builtin_amdgcn_sched_barrier(0);
  const int tile0 = ((((gi & 7) + 3 * G) & 7) * 4 + (gi >> 3)) * NT;

  if (__builtin_expect(wave < 2, 1)) {
    const int iv = wave;
    {
      const unsigned wo_ = (unsigned)((lane >> 3) * 8192 + (lane & 7) * 16);
      const GAS unsigned char* wb = (const GAS unsigned char*)wq + (size_t)(128 * (gi >> 3) + 64 * iv) * 8192 + G * 1024 + (gi & 7) * 128;
      if (iv == 0) __builtin_amdgcn_global_load_lds((const GAS unsigned*)(ws + W_EPOCH), (LAS unsigned*)(L + L_EP), 4, 0, AUX_SC1);
      else GLDS((const GAS unsigned char*)x + lane * 16, L + L_X, 0, 0);
#pragma unroll
      for (int i = 0; i < 8; ++i) GLDS(wb + (size_t)i * 65536 + wo_, L + L_WQ + iv * 8192 + i * 1024, 0, AUX_NT);
    }
    __builtin_amdgcn_s_setprio(3);
    const size_t boff = ((size_t)G * SK + (size_t)(tile0 + iv) * TK) * 256;
    const GAS unsigned char* src = (const GAS unsigned char*)kc + boff;
    const int r4 = lane >> 4, c = lane & 15;
    unsigned lo[4];
#pragma unroll
    for (int j = 0; j < 4; ++j) lo[j] = r4 * 256 + ((c ^ (4 * j + r4)) * 16);
    auto issue4 = [&](int s, int h) {
      const GAS unsigned char* p = src + h * 4096;
      LAS unsigned char* dst = L + s * TB + h * 4096;
      GLDS(p + lo[0], dst, 0, AUX_NT); GLDS(p + lo[1], dst, 1024, AUX_NT); GLDS(p + lo[2], dst, 2048, AUX_NT); GLDS(p + lo[3], dst, 3072, AUX_NT);
    };
    issue4(iv, 0); issue4(iv, 1); src += 2 * TB;
    issue4(2 + iv, 0); issue4(2 + iv, 1); src += 2 * TB;
    WAIT_VM(16); C[C_WQRDY + iv] = 1;
    int u = 2, s = 4 + iv, pub = 1, lag = 1;
    bool dead = false;
    auto block_on = [&](int word, int need) -> bool {
      WAIT_VM(0); pub = u; C[C_FULL + iv] = pub;
      bool ok; SPIN(rfl(C[word]) >= need, 1, ok);
      return ok;
    };
    auto vmin = [&]() -> int { const v4u p = *(volatile LAS v4u*)(L + L_CTRL + C_VP * 4); return rfl((int)min(min(p[0], p[1]), min(p[2], p[3]))); };
    auto block_v = [&](int need) -> bool {
      WAIT_VM(0); pub = u; C[C_FULL + iv] = pub;
      bool ok; SPIN(vmin() >= need, 1, ok);
      return ok;
    };
    int due16 = TS0 * 16 + 2 * (TSP * 16 / 10);
    unsigned org = tw0, due = org + (due16 >> 4);
    bool anch = false;
    int inc16 = TSP * 16 / 10;
#define PACE_WAIT() do { if (lag == 1) WAIT_VM(8); else WAIT_VM(16); } while (0)
    WAIT_VM(8); C[C_FULL + iv] = 1;
#pragma unroll 1
    for (; u < NT; ++u) {
      const int i = 2 * u + iv;
      if (i >= NT + NSLOT) {
        if (vmin() < i - NSLOT - NT + 1 && !block_v(i - NSLOT - NT + 1)) { dead = true; break; }
        asm volatile("" ::: "memory");
      } else if (i >= NSLOT - 2) {
        const int word = i >= NSLOT ? C_FREE + s : C_QCNT, need = i >= NSLOT ? i - NSLOT + 1 : NC;
        if (rfl(C[word]) < need && !block_on(word, need)) { dead = true; break; }
        asm volatile("" ::: "memory");
      }
      if (u >= 6) { const v2u a = *(volatile LAS v2u*)(L + L_CTRL + C_ANCH * 4); const unsigned o = (unsigned)rfl((int)a[0]); if (o) { org = o; inc16 = rfl((int)a[1]); due = org + (due16 >> 4); anch = true; } }
      if (u == NT / 2) {
        src = (const GAS unsigned char*)vc + boff;
#pragma unroll
        for (int j = 0; j < 4; ++j) lo[j] = r4 * 256 + ((c ^ (((4 * j + r4) & 7) << 1)) * 16);
      }
      unsigned now = clk32();
      if (anch && (int)(due - now) > AHD) {
        WAIT_VM(0); pub = u; C[C_FULL + iv] = pub;
        do { now = clk32(); } while ((int)(due - now) > AHD);
      }
      issue4(s, 0);
      {
        const int late = (int)(now - due);
        lag = late > 0 ? 2 : 1;
        C[C_LATE + iv] = late;
        if (u == NT - 1) C[C_DUE] = (int)(due | 1u);
        due16 += inc16;
        due = org + (due16 >> 4);
      }
      PACE_WAIT();
      issue4(s, 1);
      src += 2 * TB;
      PACE_WAIT();
      if (pub < u + 1 - lag) { pub = u + 1 - lag; C[C_FULL + iv] = pub; }
      s += 2; if (s >= NSLOT) s -= NSLOT;
    }
#undef PACE_WAIT
    if (!dead && vmin() < NT + iv - NSLOT + 1 && !block_v(NT + iv - NSLOT + 1)) dead = true;
    if (dead) { WAIT_VM(0); C[C_ABORT] = 1; return; }
    const GAS unsigned char* wb = (const GAS unsigned char*)wo + (size_t)(64 * db + 32 * iv + r4) * 1024 + cq * 256 + c * 16;
    asm volatile("" : "+v"(wb) :: "memory");
    if (pub < NT - 1) { WAIT_VM(8); C[C_FULL + iv] = NT - 1; }
    WAIT_VM(0); C[C_FULL + iv] = NT;
#pragma unroll
    for (int i = 0; i < 8; ++i) GLDS(wb + i * 4096, L + (WO_SLOT + iv) * TB + i * 1024, 0, AUX_NT);
    WAIT_VM(0);
    C[C_WORDY + iv] = 1;
    return;
  }

  const __amdgpu_buffer_rsrc_t rs = __builtin_amdgcn_make_buffer_rsrc((void*)ws, 0, (int)W_END, 0x00020000);
  if (wave < 2 + NC) {
    const int cw = wave - 2;
    SPIN_OR_QUIT(rfl(C[C_WQRDY] & C[C_WQRDY + 1]) != 0, 1);
    const unsigned twq = clk32();
    const unsigned tag = *(const LAS unsigned*)(L + L_EP) + 1u;
    {
      v4u wv[4];
      float xn[4];
#pragma unroll
      for (int k = 0; k < 4; ++k) {
        wv[k] = *(const LAS v4u*)(L + L_WQ + (4 * cw + k) * 1024 + lane * 16);
        xn[k] = bf_lo((unsigned)*(const LAS u16*)(L + L_X + (128 * (gi >> 3) + 32 * cw + 8 * k + (lane >> 3)) * 2));
      }
      const v4u xr = *(const LAS v4u*)(L + L_X + 16 * lane);
      float xv[8];
#pragma unroll
      for (int k = 0; k < 4; ++k) { xv[2 * k] = bf_lo(xr[k]); xv[2 * k + 1] = bf_hi(xr[k]); }
      float m, den;
      ln_stats(xv, m, den);
#pragma unroll
      for (int k = 0; k < 4; ++k) xn[k] = ln_apply(xn[k], m, den);
      float a[8];
#pragma unroll
      for (int k = 0; k < 8; ++k) a[k] = 0.f;
#pragma unroll
      for (int i = 0; i < 4; ++i)
#pragma unroll
        for (int k = 0; k < 4; ++k) { a[2 * k] = fmaf(xn[i], bf_lo(wv[i][k]), a[2 * k]); a[2 * k + 1] = fmaf(xn[i], bf_hi(wv[i][k]), a[2 * k + 1]); }
      const bool b3 = lane & 8;
#pragma unroll
      for (int k = 0; k < 4; ++k) { const float keep = b3 ? a[k + 4] : a[k], send = b3 ? a[k] : a[k + 4]; a[k] = keep + dpp<XOR8>(send); }
#pragma unroll
      for (int k = 0; k < 2; ++k) { auto t = __builtin_amdgcn_permlane16_swap(__float_as_uint(a[k]), __float_as_uint(a[k + 2]), false, false); a[k] = __uint_as_float(t[0]) + __uint_as_float(t[1]); }
      { auto t = __builtin_amdgcn_permlane32_swap(__float_as_uint(a[0]), __float_as_uint(a[1]), false, false); a[0] = __uint_as_float(t[0]) + __uint_as_float(t[1]); }
      const int qc = 8 * (lane & 7) + ((lane >> 3) & 1) * 4 + ((lane >> 4) & 1) * 2 + (lane >> 5);
      LAS float* QP = (LAS float*)(L + L_QP);
      QP[cw * 64 + qc] = a[0];
      WAIT_LGKM0();
      int old = 0;
      if (lane == 0) old = __hip_atomic_fetch_add((LAS int*)&C[C_QCNT], 1, __ATOMIC_RELAXED, __HIP_MEMORY_SCOPE_WORKGROUP);
      if (rfl(old) == NC - 1) {
        asm volatile("" ::: "memory");
        const v2u pq = {__float_as_uint((QP[lane] + QP[64 + lane]) + (QP[128 + lane] + QP[192 + lane])), tag};
        __builtin_amdgcn_raw_buffer_store_b64(pq, rs, W_QG + ((G * 4 + (gi >> 3)) * 512 + (gi & 7) * 64 + lane) * 8, 0, AUX_SC1);
        const v2u tq = {twq, tag};
        __builtin_amdgcn_raw_buffer_store_b64(tq, rs, lane == 0 ? W_TG + b * 8 : OOB, 0, AUX_SC1);
      }
    }

    const int g4 = lane >> 4, r16 = lane & 15;
    const int fword = C_FULL + (cw & 1);
    LAS unsigned char* SW = L + L_S + cw * 4096 + g4 * 64 + (lane & 3) * 16;
    constexpr float SCALE = 0.08838834764831845f, LOG2E = 1.4426950408889634f;
    int slot = cw;
    float smax = -INFINITY;
    {
      unsigned kaddr[4];
#pragma unroll
      for (int s = 0; s < 4; ++s) kaddr[s] = r16 * 256 + (((4 * s + g4) ^ r16) * 16);
      v4u qf[4];
      auto readK = [&](v4u (&kf)[8], int i) -> bool {
        bool ok; SPIN(rfl(C[fword]) > (i >> 1), 0, ok);
        if (!ok) return false;
        const unsigned sb = slot * TB;
#pragma unroll
        for (int s = 0; s < 4; ++s) { kf[s] = *(const LAS v4u*)(L + sb + kaddr[s]); kf[4 + s] = *(const LAS v4u*)(L + sb + kaddr[s] + 4096); }
        WAIT_LGKM0();
        C[C_FREE + slot] = i + 1;
        asm volatile("" : "+v"(kf[0]), "+v"(kf[1]), "+v"(kf[2]), "+v"(kf[3]), "+v"(kf[4]), "+v"(kf[5]), "+v"(kf[6]), "+v"(kf[7]) :: "memory");
        slot += NC; if (slot >= NSLOT) slot -= NSLOT;
        return true;
      };
      auto scores = [&](const v4u (&kf)[8], int j) {
        f32x4 s0 = {0.f, 0.f, 0.f, 0.f}, s1 = {0.f, 0.f, 0.f, 0.f};
#pragma unroll
        for (int s = 0; s < 4; ++s) { s0 = mfma16(kf[s], qf[s], s0); s1 = mfma16(kf[4 + s], qf[s], s1); }
        v4u sv;
#pragma unroll
        for (int k = 0; k < 2; ++k) {
          const unsigned u0 = pk_bf16(s0[2 * k], s0[2 * k + 1]), u1 = pk_bf16(s1[2 * k], s1[2 * k + 1]);
          sv[k] = pk_bf16(bf_lo(u0) * SCALE, bf_hi(u0) * SCALE); sv[2 + k] = pk_bf16(bf_lo(u1) * SCALE, bf_hi(u1) * SCALE);
        }
        if (r16 < 4) *(LAS v4u*)(SW + j * 256) = sv;
#pragma unroll
        for (int k = 0; k < 4; ++k) smax = fmaxf(smax, fmaxf(bf_lo(sv[k]), bf_hi(sv[k])));
      };
      v4u ka[8], kb[8], kc_[8], kd[8];
      if (!readK(ka, cw) || !readK(kc_, cw + NC) || !readK(kd, cw + 2 * NC) || !readK(kb, cw + 3 * NC)) { C[C_ABORT] = 1; return; }
      SPIN_OR_QUIT(rfl(C[C_QRDY]) != 0, 1);
#pragma unroll
      for (int s = 0; s < 4; ++s) qf[s] = *(const LAS v4u*)(L + L_Q + (lane & 3) * 256 + s * 64 + g4 * 16);
      scores(ka, 0); scores(kc_, 1); scores(kd, 2);
#pragma unroll 1
      for (int j = 3;; ++j) {
        scores(kb, j);
        if (j == NT / NC - 1) break;
        if (!readK(kb, cw + NC * (j + 1))) { C[C_ABORT] = 1; return; }
      }
    }
    float inv;
    unsigned lse;
    {
      const float mw = x32_max(x16_max(smax));
      if (lane < 4) *(LAS float*)(L + L_ML + (lane * 4 + cw) * 4) = mw;
      WAIT_LGKM0();
      if (lane == 0) __hip_atomic_fetch_add((LAS int*)&C[C_XCNT], 1, __ATOMIC_RELAXED, __HIP_MEMORY_SCOPE_WORKGROUP);
      SPIN_OR_QUIT(rfl(C[C_XCNT]) >= NC, 0);
      const f32x4 m4 = *(LAS f32x4*)(L + L_ML + (lane & 3) * 16);
      const float M = fmaxf(fmaxf(m4[0], m4[1]), fmaxf(m4[2], m4[3])) * LOG2E;
      float lsum = 0.f;
#pragma unroll
      for (int jj = 0; jj < 4; ++jj) {
        LAS v4u* sp = (LAS v4u*)(SW + (4 * jj + (r16 >> 2)) * 256);
        const v4u sv = *sp;
        float pr[8];
#pragma unroll
        for (int k = 0; k < 4; ++k) { pr[2 * k] = __builtin_amdgcn_exp2f(fmaf(bf_lo(sv[k]), LOG2E, -M)); pr[2 * k + 1] = __builtin_amdgcn_exp2f(fmaf(bf_hi(sv[k]), LOG2E, -M)); }
        lsum += ((pr[0] + pr[1]) + (pr[2] + pr[3])) + ((pr[4] + pr[5]) + (pr[6] + pr[7]));
        *sp = v4u{pk_bf16(pr[0], pr[1]), pk_bf16(pr[2], pr[3]), pk_bf16(pr[4], pr[5]), pk_bf16(pr[6], pr[7])};
      }
      lsum += dpp<ROR4>(lsum); lsum += dpp<XOR8>(lsum);
      lsum = x32_sum(x16_sum(lsum));
      if (lane < 4) *(LAS float*)(L + L_ML + 64 + (lane * 4 + cw) * 4) = lsum;
      WAIT_LGKM0();
      if (lane == 0) __hip_atomic_fetch_add((LAS int*)&C[C_LCNT], 1, __ATOMIC_RELAXED, __HIP_MEMORY_SCOPE_WORKGROUP);
      SPIN_OR_QUIT(rfl(C[C_LCNT]) >= NC, 0);
      const f32x4 l4 = *(LAS f32x4*)(L + L_ML + 64 + (lane & 3) * 16);
      const float Ls = (l4[0] + l4[1]) + (l4[2] + l4[3]);
      inv = __builtin_amdgcn_rcpf(Ls);
      lse = __float_as_uint(M + __builtin_amdgcn_logf(Ls));
    }
    {
      f32x4 O0 = {0.f, 0.f, 0.f, 0.f}, O1 = {0.f, 0.f, 0.f, 0.f};
      const int vrow = 4 * g4 + ((lane >> 2) & 3);
      const unsigned va0 = (vrow * 256 + (vrow & 7) * 32 + (lane & 3) * 8) ^ (cw * 64);
      const unsigned pa0 = L_S + g4 * 64 + (lane & 3) * 16;
      int vs = NT % NSLOT, avail = 0;
#pragma unroll 1
      for (int t = 0; t < NT; t += 2) {
        v4u pf0 = *(const LAS v4u*)(L + pa0 + (t & 3) * 4096 + (t >> 2) * 256), pf1 = *(const LAS v4u*)(L + pa0 + (t & 3) * 4096 + 4096 + (t >> 2) * 256);
        const int vs1 = (vs + 1 == NSLOT) ? 0 : vs + 1;
        unsigned a = vs * TB + va0, c = vs1 * TB + va0;
        asm volatile("" : "+v"(pf0), "+v"(pf1), "+v"(a), "+v"(c));
        if (t >= avail) {
          auto poll = [&]() -> bool {
            const v2u f = *(volatile LAS v2u*)(L + L_CTRL + C_FULL * 4);
            avail = 2 * rfl((int)min(f[0], f[1])) - NT;
            return avail > t;
          };
          SPIN_OR_QUIT(poll(), 0);
        }
        auto frag = [&](unsigned ad) -> v4u {
          const v2u lo = __builtin_bit_cast(v2u, __builtin_amdgcn_ds_read_tr16_b64_v4i16((LAS s16x4*)(L + ad)));
          const v2u hi = __builtin_bit_cast(v2u, __builtin_amdgcn_ds_read_tr16_b64_v4i16((LAS s16x4*)(L + ad + 4096)));
          return v4u{lo[0], lo[1], hi[0], hi[1]};
        };
        const v4u f0 = frag(a), f1 = frag(a ^ 32), f2 = frag(c), f3 = frag(c ^ 32);
        O0 = mfma16(f0, pf0, O0);
        O1 = mfma16(f1, pf0, O1);
        O0 = mfma16(f2, pf1, O0);
        O1 = mfma16(f3, pf1, O1);
        WAIT_LGKM0();
        C[C_VP + cw] = t + 2;
        vs = (vs1 + 1 == NSLOT) ? 0 : vs1 + 1;
      }
      const int cg = r16 >> 2;
      const f32x4 Os = (cg & 2) ? O1 : O0;
      const float o0 = (cg & 1) ? Os[2] : Os[0], o1 = (cg & 1) ? Os[3] : Os[1];
      const v4u gr = {__float_as_uint(o0 * inv), (tag << 16) | (lse >> 16), __float_as_uint(o1 * inv), (tag << 16) | (lse & 0xffffu)};
      __builtin_amdgcn_raw_buffer_store_b128(gr, rs, W_P1 + ((4 * G + (lane & 3)) * 32 + gi) * 1024 + (16 * cw + 8 * (cg >> 1) + 2 * g4 + (cg & 1)) * 16, 0, AUX_SC1);
      C[C_MCNT] = NC;
    }
    v4u pd[4];
    unsigned wp[8][2];
    {
      const unsigned t16 = tag & 0xffffu;
      unsigned po[4];
#pragma unroll
      for (int i = 0; i < 4; ++i) { po[i] = W_P1 + ((db >> 1) * 32 + 8 * i + (lane >> 3)) * 1024 + ((db & 1) * 4 + cw) * 128 + (lane & 7) * 16; pd[i] = v4u{0u, 0u, 0u, 0u}; }
      { const unsigned g = (unsigned)rfl(C[C_DUE] + C[C_GX]) + P1D; while ((int)(g - clk32()) > 0) __builtin_amdgcn_s_sleep(1); }
      bool havew = false, wfail = false;
      auto pass = [&]() -> bool {
        v4u n[4];
#pragma unroll
        for (int i = 0; i < 4; ++i) n[i] = __builtin_amdgcn_raw_buffer_load_b128(rs, po[i], 0, AUX_SC1);
        if (!havew) {
          bool okw; SPIN(rfl(C[C_WORDY + (cw >> 1)]) != 0, 0, okw);
          havew = true;
          const LAS unsigned* wsrc = (const LAS unsigned*)(L + WO_SLOT * TB + cw * 4096) + lane;
#pragma unroll
          for (int j = 0; j < 8; ++j) {
            const unsigned wa = wsrc[(2 * j) * 64], wb = wsrc[(2 * j + 1) * 64];
            wp[j][0] = __builtin_amdgcn_perm(wb, wa, 0x05040100u);
            wp[j][1] = __builtin_amdgcn_perm(wb, wa, 0x07060302u);
          }
          if (!okw) wfail = true;
        }
#pragma unroll
        for (int i = 0; i < 4; ++i) {
          if (po[i] != OOB) pd[i] = n[i];
          if ((n[i][1] >> 16) == t16 && (n[i][3] >> 16) == t16) po[i] = OOB;
        }
        return __all((po[0] & po[1] & po[2] & po[3]) == OOB);
      };
      SPIN_OR_QUIT(pass(), 0);
      if (wfail) { C[C_ABORT] = 1; return; }
    }
    {
      float e[4];
#pragma unroll
      for (int i = 0; i < 4; ++i) e[i] = __uint_as_float(__builtin_amdgcn_perm(pd[i][1], pd[i][3], 0x05040100u));
      float M = fmaxf(fmaxf(e[0], e[1]), fmaxf(e[2], e[3]));
      M = fmaxf(M, dpp<XOR8>(M)); M = x32_max(x16_max(M));
      float Ls = 0.f, oa = 0.f, ob = 0.f;
#pragma unroll
      for (int i = 0; i < 4; ++i) { const float w = __builtin_amdgcn_exp2f(e[i] - M); Ls += w; oa = fmaf(w, __uint_as_float(pd[i][0]), oa); ob = fmaf(w, __uint_as_float(pd[i][2]), ob); }
      Ls += dpp<XOR8>(Ls); oa += dpp<XOR8>(oa); ob += dpp<XOR8>(ob);
      Ls = x32_sum(x16_sum(Ls)); oa = x32_sum(x16_sum(oa)); ob = x32_sum(x16_sum(ob));
      const float inv = __builtin_amdgcn_rcpf(Ls);
      const unsigned op = pk_bf16(oa * inv, ob * inv);
      float ya = 0.f, yb = 0.f, yc = 0.f, yd = 0.f;
#pragma unroll
      for (int j = 0; j < 8; j += 2) {
        const unsigned o0 = (unsigned)__builtin_amdgcn_readlane((int)op, j), o1 = (unsigned)__builtin_amdgcn_readlane((int)op, j + 1);
        ya = dot2(wp[j][0], o0, ya); yb = dot2(wp[j][1], o0, yb); yc = dot2(wp[j + 1][0], o1, yc); yd = dot2(wp[j + 1][1], o1, yd);
      }
      LAS f32x2* Y = (LAS f32x2*)(L + L_Y);
      Y[cw * 64 + lane] = f32x2{ya + yc, yb + yd};
      WAIT_LGKM0();
      int old = 0;
      if (lane == 0) old = __hip_atomic_fetch_add((LAS int*)&C[C_YCNT], 1, __ATOMIC_RELAXED, __HIP_MEMORY_SCOPE_WORKGROUP);
      if (rfl(old) == NC - 1) {
        asm volatile("" ::: "memory");
        const f32x2 ys = (Y[lane] + Y[64 + lane]) + (Y[128 + lane] + Y[192 + lane]);
        const v4u ga = {__float_as_uint(ys[0]), tag, __float_as_uint(ys[1]), tag};
        __builtin_amdgcn_raw_buffer_store_b128(ga, rs, W_P2 + ((cq * 8 + (lane >> 3)) * 64 + db) * 128 + (lane & 7) * 16, 0, AUX_SC1);
      }
    }
    return;
  }

  SPIN_OR_QUIT(rfl(C[C_WQRDY] & C[C_WQRDY + 1]) != 0, 1);
  const unsigned tag = *(const LAS unsigned*)(L + L_EP) + 1u;
  unsigned org0;
  {
    { const unsigned tq = clk32(); while (clk32() - tq < QDELAY) __builtin_amdgcn_s_sleep(2); }
    v4u g[16];
    v2u tg;
    auto pass = [&]() -> bool {
      tg = __builtin_amdgcn_raw_buffer_load_b64(rs, W_TG + (G * 32 + (lane & 31)) * 8, 0, AUX_SC1);
#pragma unroll
      for (int j = 0; j < 16; ++j) g[j] = __builtin_amdgcn_raw_buffer_load_b128(rs, W_QG + G * 16384 + j * 1024 + lane * 16, 0, AUX_SC1);
      __builtin_amdgcn_sched_barrier(0);
      unsigned bad = tg[1] ^ tag;
#pragma unroll
      for (int j = 0; j < 16; ++j) bad |= (g[j][1] ^ tag) | (g[j][3] ^ tag);
      return __all(bad == 0u);
    };
    SPIN_OR_QUIT(pass(), 1);
#pragma unroll
    for (int j = 0; j < 4; ++j) {
      const float q0 = (__uint_as_float(g[j][0]) + __uint_as_float(g[4 + j][0])) + (__uint_as_float(g[8 + j][0]) + __uint_as_float(g[12 + j][0]));
      const float q1 = (__uint_as_float(g[j][2]) + __uint_as_float(g[4 + j][2])) + (__uint_as_float(g[8 + j][2]) + __uint_as_float(g[12 + j][2]));
      *(LAS unsigned*)(L + L_Q + j * 256 + lane * 4) = pk_bf16(q0, q1);
    }
    WAIT_LGKM0();
    C[C_QRDY] = 1;
    {
      const float dm = fminf(fmaxf(wave_sum((float)(int)(tg[0] - tw0)) * (1.f / 64.f), -2000.f), 2000.f);
      org0 = (unsigned)rfl((int)(tw0 + (unsigned)(int)dm - ANCH0));
      *(volatile LAS v2u*)(L + L_CTRL + C_ANCH * 4) = v2u{org0 | 1u, (unsigned)(TSP * 16 / 10)};
    }
  }
  int slip = 0, exc = 0;
  auto ckpt = [&](int k) {
    const int T = CK0 + CKD * k;
    while ((int)(clk32() - org0) < T) __builtin_amdgcn_s_sleep(4);
    const v2u lt = *(volatile LAS v2u*)(L + L_CTRL + C_LATE * 4);
    const int l0 = min(max((int)lt[0], -8000), 8000), l1 = min(max((int)lt[1], -8000), 8000);
    const v2u rep = {((unsigned)(l0 + l1) & 0xffffu) | ((unsigned)max(l0, l1) << 16), tag};
    __builtin_amdgcn_raw_buffer_store_b64(rep, rs, (lane == 0 && b < 64) ? W_CK + (k * 64 + b) * 8 : OOB, 0, AUX_SC1);
    while ((int)(clk32() - org0) < T + CKW) __builtin_amdgcn_s_sleep(4);
    asm volatile("" ::: "memory");
    const v2u e = __builtin_amdgcn_raw_buffer_load_b64(rs, W_CK + (k * 64 + lane) * 8, 0, AUX_SC1);
    const bool val = e[1] == tag;
    const float n = wave_sum(val ? 1.f : 0.f), sum = wave_sum(val ? (float)(int)(short)(e[0] & 0xffffu) : 0.f);
    const int lm = rfl((int)(sum / fmaxf(2.f * n, 1.f)));
    if (k == NCK - 1) {
      float mx = val ? (float)((int)e[0] >> 16) : -1e9f;
      mx = fmaxf(mx, dpp<XOR1>(mx)); mx = fmaxf(mx, dpp<XOR2>(mx)); mx = fmaxf(mx, dpp<XOR7>(mx)); mx = fmaxf(mx, dpp<XOR8>(mx)); mx = x32_max(x16_max(mx));
      C[C_GX] = min(max(rfl((int)mx) - GXT, 0), 300);
    } else if (rfl((int)n) >= 48 && lm > CKTH) {
      exc += lm;
      const int e16 = exc * 16 * TSP / (T * 10);
      slip += lm + CKM + e16 / 4;
      *(volatile LAS v2u*)(L + L_CTRL + C_ANCH * 4) = v2u{(org0 + (unsigned)slip) | 1u, (unsigned)(TSP * 16 / 10 + e16)};
      exc += (e16 * CKD * 10 / TSP) >> 4;
    }
  };
  {
    const v4u xr = *(const LAS v4u*)(L + L_X + 16 * lane);
    float xv[8];
#pragma unroll
    for (int k = 0; k < 4; ++k) { xv[2 * k] = bf_lo(xr[k]); xv[2 * k + 1] = bf_hi(xr[k]); }
    float m, den, m2, den2;
    ln_stats(xv, m, den);
#pragma unroll
    for (int k = 0; k < 8; ++k) xv[k] = ln_apply(xv[k], m, den);
    ln_stats(xv, m2, den2);
    const unsigned xd = *(const LAS unsigned*)(L + L_X + 4 * b);
    const float h0 = ln_apply(ln_apply(bf_lo(xd), m, den), m2, den2), h1 = ln_apply(ln_apply(bf_hi(xd), m, den), m2, den2);
    float a0 = 0.f, a1 = 0.f;
    auto gelu = [](float u) {
      const float z2 = -2.f * 0.7978845608028654f * 1.4426950408889634f * fmaf(0.044715f * u * u, u, u);
      return rbf(u * __builtin_amdgcn_rcpf(1.f + __builtin_amdgcn_exp2f(z2)));
    };
    v2u na = *(const GAS v2u*)(w1 + 4 * lane), nb = *(const GAS v2u*)(b1 + 4 * lane), nc = *(const GAS v2u*)(w2 + 4 * lane);
#pragma unroll 1
    for (int i = 1; i <= 7; ++i) {
      const v2u a = na, bb = nb, cc = nc;
      const int f = 4 * (lane + 64 * (i < 7 ? i : 0));
      na = *(const GAS v2u*)(w1 + f); nb = *(const GAS v2u*)(b1 + f); nc = *(const GAS v2u*)(w2 + f);
#pragma unroll
      for (int k = 0; k < 2; ++k) {
        a0 = fmaf(gelu(rbf(rbf(h0 * bf_lo(a[k])) + bf_lo(bb[k]))), bf_lo(cc[k]), a0);
        a0 = fmaf(gelu(rbf(rbf(h0 * bf_hi(a[k])) + bf_hi(bb[k]))), bf_hi(cc[k]), a0);
        a1 = fmaf(gelu(rbf(rbf(h1 * bf_lo(a[k])) + bf_lo(bb[k]))), bf_lo(cc[k]), a1);
        a1 = fmaf(gelu(rbf(rbf(h1 * bf_hi(a[k])) + bf_hi(bb[k]))), bf_hi(cc[k]), a1);
      }
    }
    const float bias2 = bf_lo((unsigned)b2[0]);
    const float y0 = rbf(rbf(wave_sum(a0)) + bias2), y1 = rbf(rbf(wave_sum(a1)) + bias2);
    const unsigned gates = pk_bf16(y0 / (1.f + __expf(-y0)), y1 / (1.f + __expf(-y1)));
    if (lane == 0) __hip_atomic_store((GAS unsigned long long*)(ws + W_GT) + b, ((unsigned long long)tag << 32) | gates, __ATOMIC_RELAXED, __HIP_MEMORY_SCOPE_AGENT);
  }
#pragma unroll 1
  for (int k = 0; k < NCK; ++k) ckpt(k);
  if ((b & 7) != 0) return;
  const int r = b >> 3;
  SPIN_OR_QUIT(rfl(C[C_MCNT]) >= NC, 4);
  unsigned gates;
  {
    unsigned long long gg;
    auto pass = [&]() -> bool {
      gg = __hip_atomic_load((GAS unsigned long long*)(ws + W_GT) + b + (lane & 7), __ATOMIC_RELAXED, __HIP_MEMORY_SCOPE_AGENT);
      return __all((unsigned)(gg >> 32) == tag);
    };
    SPIN_OR_QUIT(pass(), 1);
    gates = (unsigned)gg;
  }
  const unsigned xo = *(const LAS unsigned*)(L + L_X + 32 * r + 4 * (lane & 7));
  const __amdgpu_buffer_rsrc_t ors = __builtin_amdgcn_make_buffer_rsrc((void*)out, 0, D * 2, 0x00020000);
  {
    v4u g[8];
    unsigned po[8];
#pragma unroll
    for (int i = 0; i < 8; ++i) { po[i] = W_P2 + r * 8192 + i * 1024 + lane * 16; g[i] = v4u{0u, 0u, 0u, 0u}; }
    asm volatile("" : "+v"(po[0]), "+v"(po[1]), "+v"(po[2]), "+v"(po[3]), "+v"(po[4]), "+v"(po[5]), "+v"(po[6]), "+v"(po[7]));
    SPIN_OR_QUIT(rfl(C[C_YCNT]) >= NC, 0);
    { const unsigned t0 = clk32(); while (clk32() - t0 < P2D) {} }
    auto pass = [&]() -> bool {
      v4u n[8];
#pragma unroll
      for (int i = 0; i < 8; ++i) n[i] = __builtin_amdgcn_raw_buffer_load_b128(rs, po[i], 0, AUX_SC1);
      __builtin_amdgcn_sched_barrier(0);
      unsigned all = OOB;
#pragma unroll
      for (int i = 0; i < 8; ++i) {
        if (po[i] != OOB) g[i] = n[i];
        if (n[i][1] == tag && n[i][3] == tag) po[i] = OOB;
        all &= po[i];
      }
      return __all(all == OOB);
    };
    SPIN_OR_QUIT(pass(), 0);
    float s0 = 0.f, s1 = 0.f;
#pragma unroll
    for (int i = 0; i < 8; ++i) { s0 += __uint_as_float(g[i][0]); s1 += __uint_as_float(g[i][2]); }
    s0 += dpp<XOR8>(s0); s1 += dpp<XOR8>(s1);
    s0 = x32_sum(x16_sum(s0)); s1 = x32_sum(x16_sum(s1));
    const unsigned res = pk_bf16(bf_lo(xo) + rbf(rbf(s0) * bf_lo(gates)), bf_hi(xo) + rbf(rbf(s1) * bf_hi(gates)));
    __builtin_amdgcn_raw_buffer_store_b32(res, ors, lane >= 8 ? OOB : 32 * r + 4 * lane, 0, 0);
    __builtin_amdgcn_raw_buffer_store_b32(tag, rs, (b != 0 || lane != 0) ? OOB : W_EPOCH, 0, AUX_SC1);
  }
}
}

extern "C" void kernel_launch(void* const* d_in, const int* in_sizes, int n_in, void* d_out, int out_size, void* d_ws, size_t ws_size,
                              hipStream_t stream) {
  (void)hipFuncSetAttribute((const void*)fused_decode, hipFuncAttributeMaxDynamicSharedMemorySize, LDS_BYTES);
  hipLaunchKernelGGL(fused_decode, dim3(NWG), dim3(NTHR), LDS_BYTES, stream, (const u16*)d_in[0], (const u16*)d_in[1], (const u16*)d_in[2],
                     (const u16*)d_in[3], (const u16*)d_in[4], (const u16*)d_in[6], (const u16*)d_in[7], (const u16*)d_in[8],
                     (const u16*)d_in[9], (u16*)d_out, (unsigned char*)d_ws);
}
```
